```python
import torch

def _softmax(x, axis=-1):
    return torch.softmax(x, dim=axis)
def _gelu(x):
    return torch.nn.functional.gelu(x, approximate="tanh")
def _silu(x):
    return torch.nn.functional.silu(x)
def _sigmoid(x):
    return torch.sigmoid(x)
def _layernorm(x, axis=-1, eps=1e-5):
    m = x.mean(dim=axis, keepdim=True)
    v = x.var(dim=axis, keepdim=True, unbiased=False)
    return (x - m) / torch.sqrt(v + eps)
def _scatter_add(tgt, idx, src, axis=0):
    return tgt.clone().index_add_(axis, idx, src)
def _segment_sum(src, seg, n):
    out = torch.zeros((n,) + src.shape[1:], dtype=src.dtype)
    return out.index_add_(0, seg, src)
def _pad(x, pw):
    flat = tuple(v for pair in reversed(pw) for v in pair)
    return torch.nn.functional.pad(x, flat)

kernel_name = 'concat_heads__ssm_conv_s4d__mha_block__prompt'

def setup_inputs():

    _w = (lambda a: (lambda t: t.to(torch.bfloat16) if t.is_floating_point() else t)(torch.as_tensor(a)))
    def _A_setup():
        Bsz, L, D, N = 1, 2048, 512, 128
        return {
            "u": _w((lambda *s: torch.randn(*s, dtype=torch.bfloat16) * 0.05)(Bsz, L, D)),
            "log_A_real": _w((lambda *s: torch.randn(*s, dtype=torch.bfloat16) * 6.4)(D, N) * 0.5),
            "A_imag": _w((lambda *s: torch.randn(*s, dtype=torch.bfloat16) * 0.1)(D, N) * 3.14),
            "C_real": _w((lambda *s: torch.randn(*s, dtype=torch.bfloat16) * 0.4)(D, N) / N ** 0.5),
            "C_imag": _w((lambda *s: torch.randn(*s, dtype=torch.bfloat16) * 0.4)(D, N) / N ** 0.5),
            "log_dt": _w((lambda *s: torch.randn(*s, dtype=torch.bfloat16) * 0.1)(D) * 0.5 - 4.0),
            "D_skip": _w((lambda *s: torch.randn(*s, dtype=torch.bfloat16) * 0.1)(D)),
        }
    _A_in = _A_setup()

    def _B_setup():
        B, S, D, H = 1, 2048, 512, 32
        return {
            "x": _w((lambda *s: torch.randn(*s, dtype=torch.bfloat16) * 0.2)(B, S, D)),
            "wqkv": _w((lambda *s: torch.randn(*s, dtype=torch.bfloat16) * 0.2)(D, 3 * D)),
            "wo": _w((lambda *s: torch.randn(*s, dtype=torch.bfloat16) * 0.05)(D, D)),
        }
    _B_in = _B_setup()

    return {
        "A_u": _A_in["u"],
        "A_log_A_real": _A_in["log_A_real"],
        "A_A_imag": _A_in["A_imag"],
        "A_C_real": _A_in["C_real"],
        "A_C_imag": _A_in["C_imag"],
        "A_log_dt": _A_in["log_dt"],
        "A_D_skip": _A_in["D_skip"],
        "B_x": _B_in["x"],
        "B_wqkv": _B_in["wqkv"],
        "B_wo": _B_in["wo"],
    }

def reference(A_u, A_log_A_real, A_A_imag, A_C_real, A_C_imag, A_log_dt, A_D_skip, B_x, B_wqkv, B_wo):
    def _A_ref(u, log_A_real, A_imag, C_real, C_imag, log_dt, D_skip):
        L = u.shape[1]
        dt = torch.exp(log_dt)
        A = -torch.exp(log_A_real) + 1j * A_imag
        C = (C_real + 1j * C_imag) * dt[:, None]
        pos = (lambda x, dt: x.to(dt))(torch.arange(L), log_dt.dtype)
        P = torch.exp((A * dt[:, None])[:, :, None] * pos[None, None, :])
        K = 2.0 * torch.einsum("dn,dnl->dl", C, P).real
        L2 = 2 * L
        u_t = (lambda x, *d: x.permute(*d))(u, 0, 2, 1)
        Kf = (lambda x, *a, **k: torch.fft.rfft(x.float(), *a, **k))(K, L2)
        Uf = (lambda x, *a, **k: torch.fft.rfft(x.float(), *a, **k))(u_t, L2)
        y = (lambda x, *a, **k: torch.fft.irfft(x, *a, **k).to(torch.bfloat16))(Uf * Kf[None, :, :], L2)[:, :, :L]
        y = (lambda x, *d: x.permute(*d))(y, 0, 2, 1)
        return y + u * D_skip[None, None, :]

    def _B_ref(x, wqkv, wo):
        B, S, D = x.shape
        H = 32
        Dh = D // H
        qkv = (lambda x, *d: x.permute(*d))((x @ wqkv).reshape(B, S, 3, H, Dh), 2, 0, 3, 1, 4)
        q, k, v = qkv[0], qkv[1], qkv[2]
        scores = torch.einsum("bhqd,bhkd->bhqk", q, k) * (Dh ** -0.5)
        p = _softmax(scores, axis=-1)
        o = torch.einsum("bhqk,bhkd->bhqd", p, v)
        return (lambda x, *d: x.permute(*d))(o, 0, 2, 1, 3).reshape(B, S, D) @ wo

    a = _A_ref(A_u, A_log_A_real, A_A_imag, A_C_real, A_C_imag, A_log_dt, A_D_skip)
    b = _B_ref(B_x, B_wqkv, B_wo)
    return torch.concatenate([a, b], axis=-1)
```

```cpp
#include <hip/hip_runtime.h>
#include <hip/hip_bf16.h>
#include <stdint.h>

typedef __bf16 bf16x8 __attribute__((ext_vector_type(8)));
typedef __bf16 bf16x2 __attribute__((ext_vector_type(2)));
typedef _Float16 f16x8 __attribute__((ext_vector_type(8)));
typedef _Float16 f16x2 __attribute__((ext_vector_type(2)));
typedef float f32x16 __attribute__((ext_vector_type(16)));
typedef float f32x2 __attribute__((ext_vector_type(2)));
typedef unsigned u32x4 __attribute__((ext_vector_type(4)));
typedef unsigned u32x4a __attribute__((ext_vector_type(4), aligned(4)));
typedef unsigned u32x2 __attribute__((ext_vector_type(2)));
typedef short s16x4 __attribute__((ext_vector_type(4)));
#define LDSP __attribute__((address_space(3)))
#define DI __device__ __forceinline__

constexpr int SEQ = 2048, DM = 512, NH = 32;
constexpr unsigned VTILE = 1040;
constexpr unsigned VHEAD = 64 * VTILE;
constexpr unsigned TAP_C1 = 4288, TAP_CH = 8512, TAP_IMG = 17408, TAP_USED = 17024;
constexpr size_t WS_Q = 0;
constexpr size_t WS_K = WS_Q + (size_t)NH * 65536;
constexpr size_t WS_V = WS_K + (size_t)NH * 65536;
constexpr size_t WS_KMAX = WS_V + (size_t)NH * VHEAD;
constexpr size_t WS_TAPS = WS_KMAX + (size_t)NH * 64 * 4;
constexpr size_t WS_O = WS_TAPS + (size_t)256 * TAP_IMG;
constexpr float QSCALE = 0.25f * 1.4426950408889634f;
constexpr float SAFE_BOUND = 40.0f;

DI float bf2f(unsigned short v) { return __uint_as_float((unsigned)v << 16); }
DI unsigned cvtpk_bf16(float lo, float hi) { f32x2 v = {lo, hi}; return __builtin_bit_cast(unsigned, __builtin_convertvector(v, bf16x2)); }
DI unsigned cvtpk_f16(float lo, float hi) { f32x2 v = {lo, hi}; return __builtin_bit_cast(unsigned, __builtin_convertvector(v, f16x2)); }
DI float bf16r(float x) { return __uint_as_float(cvtpk_bf16(x, x) << 16); }
DI f32x16 mfma_bf16(bf16x8 a, bf16x8 b, f32x16 c) { return __builtin_amdgcn_mfma_f32_32x32x16_bf16(a, b, c, 0, 0, 0); }
DI f32x16 mfma_f16(f16x8 a, f16x8 b, f32x16 c) { return __builtin_amdgcn_mfma_f32_32x32x16_f16(a, b, c, 0, 0, 0); }
DI bf16x8 as_bf(u32x4 v) { return __builtin_bit_cast(bf16x8, v); }
DI void glds16(const void* sbase, unsigned voff, unsigned lds) {
    unsigned sv;
    asm volatile("s_mov_b32 %0, m0\n\ts_mov_b32 m0, %3\n\ts_nop 0\n\tglobal_load_lds_dwordx4 %1, %2\n\ts_mov_b32 m0, %0"
                 : "=&s"(sv) : "v"(voff), "s"(sbase), "s"(lds) : "memory");
}
#define WAIT_VM(n) asm volatile("s_waitcnt vmcnt(" #n ")" ::: "memory")
#define WAIT_BAR(n) asm volatile("s_waitcnt vmcnt(" #n ") lgkmcnt(0)\n\ts_barrier" ::: "memory")
#define BAR_LGKM() asm volatile("s_waitcnt lgkmcnt(0)\n\ts_barrier" ::: "memory")
template <int CTRL> DI float dpp_max(float v) {
    const int o = __builtin_amdgcn_update_dpp(__float_as_int(v), __float_as_int(v), CTRL, 0xf, 0xf, false);
    return fmaxf(v, __int_as_float(o));
}
DI float wave_max(float v) {
    v = dpp_max<0x111>(v); v = dpp_max<0x112>(v); v = dpp_max<0x114>(v); v = dpp_max<0x118>(v);
    v = dpp_max<0x142>(v); v = dpp_max<0x143>(v);
    return __int_as_float(__builtin_amdgcn_readlane(__float_as_int(v), 63));
}
DI float half_sum(float v) {
    auto r = __builtin_amdgcn_permlane32_swap(__float_as_uint(v), __float_as_uint(v), false, false);
    return __uint_as_float(r[0]) + __uint_as_float(r[1]);
}
DI float half_max(float v) {
    auto r = __builtin_amdgcn_permlane32_swap(__float_as_uint(v), __float_as_uint(v), false, false);
    return fmaxf(__uint_as_float(r[0]), __uint_as_float(r[1]));
}
DI u32x4 pack8(const f32x16& a, int o, float s) {
    u32x4 r;
    r[0] = cvtpk_bf16(a[o + 0] * s, a[o + 1] * s); r[1] = cvtpk_bf16(a[o + 2] * s, a[o + 3] * s);
    r[2] = cvtpk_bf16(a[o + 4] * s, a[o + 5] * s); r[3] = cvtpk_bf16(a[o + 6] * s, a[o + 7] * s);
    return r;
}
DI bf16x8 tr_frag(const LDSP char* p, int off_lo, int off_hi) {
    const s16x4 lo = __builtin_amdgcn_ds_read_tr16_b64_v4i16((LDSP s16x4*)(p + off_lo));
    const s16x4 hi = __builtin_amdgcn_ds_read_tr16_b64_v4i16((LDSP s16x4*)(p + off_hi));
    typedef short s16x8 __attribute__((ext_vector_type(8)));
    const s16x8 v = __builtin_shufflevector(lo, hi, 0, 1, 2, 3, 4, 5, 6, 7);
    return __builtin_bit_cast(bf16x8, v);
}

constexpr unsigned K1_STAGE = 28672, K1_W = 16384;
constexpr unsigned K1_SSM = 4 * K1_STAGE;
constexpr unsigned K1_ROW = 272, K1_BUF = 32 * K1_ROW;
constexpr unsigned K1_TAB = K1_SSM + 4 * K1_BUF;
constexpr unsigned K1_LDS = K1_TAB + 2 * 644 * 4;

struct cplx { float r, i; };
DI cplx cmul(cplx a, cplx b) { cplx o; o.r = a.r * b.r - a.i * b.i; o.i = a.r * b.i + a.i * b.r; return o; }
DI cplx zpow(float xl, float yr, float p) {
    const float e = __builtin_amdgcn_exp2f(xl * p);
    const float th = __builtin_amdgcn_fractf(yr * p);
    cplx o; o.r = e * __builtin_amdgcn_cosf(th); o.i = e * __builtin_amdgcn_sinf(th); return o;
}

__global__ __launch_bounds__(512) void k1_qkv_taps(const unsigned short* __restrict__ x, const unsigned short* __restrict__ wqkv,
                                                   const unsigned short* __restrict__ logA, const unsigned short* __restrict__ aim,
                                                   const unsigned short* __restrict__ cre, const unsigned short* __restrict__ cim,
                                                   const unsigned short* __restrict__ logdt, char* __restrict__ ws) {
    __shared__ __attribute__((aligned(16))) char smem[K1_LDS];
    LDSP char* const sm = (LDSP char*)smem;
    const unsigned lds0 = (unsigned)(uintptr_t)sm;
    const int lane = threadIdx.x & 63, w = __builtin_amdgcn_readfirstlane(threadIdx.x >> 6), b = blockIdx.x;
    const int r = lane & 31, h = lane >> 5;
    if (w < 4) {
        const int g = b & 7, idx = b >> 3, tb = (g & 3) * 4 + (idx & 3), hp = (g >> 2) * 8 + (idx >> 2);
        unsigned xoff[4];
#pragma unroll
        for (int i = 0; i < 4; ++i) { const int rr = 8 * i + (lane >> 3); xoff[i] = rr * 1024 + (((lane & 7) ^ ((rr >> 1) & 7)) << 4); }
        const unsigned woff = (lane >> 2) * 3072 + (lane & 3) * 16;
        const char* xb = (const char*)x + (size_t)(tb * 128 + w * 32) * 1024;
        const char* wb = (const char*)wqkv + (size_t)(w * 16) * 3072 + hp * 64;
        auto issue = [&](int s) {
            const unsigned slot = lds0 + (s & 3) * K1_STAGE;
#pragma unroll
            for (int i = 0; i < 4; ++i) glds16(xb + s * 128, xoff[i], slot + w * 4096 + i * 1024);
#pragma unroll
            for (int j = 0; j < 3; ++j) glds16(wb + (size_t)s * 64 * 3072 + j * 1024, woff, slot + K1_W + j * 4096 + w * 1024);
        };
        issue(0); issue(1); issue(2);
        unsigned xa[4];
#pragma unroll
        for (int t = 0; t < 4; ++t) xa[t] = (w * 32 + r) * 128 + (((2 * t + h) ^ ((r >> 1) & 7)) << 4);
        const unsigned wa = K1_W + (8 * h + ((lane & 15) >> 2)) * 64 + (((lane >> 4) & 1) * 16 + (lane & 3) * 4) * 2;
        f32x16 aq = {}, ak = {}, av = {};
#pragma unroll
        for (int s = 0; s < 8; ++s) {
            if (s <= 5) WAIT_BAR(14); else if (s == 6) WAIT_BAR(7); else WAIT_BAR(0);
            if (s + 3 < 8) issue(s + 3);
            const LDSP char* sb = sm + (s & 3) * K1_STAGE;
#pragma unroll
            for (int t = 0; t < 4; ++t) {
                const bf16x8 xf = *(const LDSP bf16x8*)(sb + xa[t]);
                const bf16x8 wq = tr_frag(sb + wa, t * 1024, t * 1024 + 256);
                const bf16x8 wk = tr_frag(sb + wa, 4096 + t * 1024, 4096 + t * 1024 + 256);
                const bf16x8 wv = tr_frag(sb + wa, 8192 + t * 1024, 8192 + t * 1024 + 256);
                aq = mfma_bf16(wq, xf, aq);
                ak = mfma_bf16(wk, xf, ak);
                av = mfma_bf16(xf, wv, av);
            }
        }
        const int qt = tb * 4 + w, hA = 2 * hp;
        *(u32x4*)(ws + WS_Q + ((size_t)hA * 64 + qt) * 1024 + lane * 16) = pack8(aq, 0, QSCALE);
        *(u32x4*)(ws + WS_Q + ((size_t)(hA + 1) * 64 + qt) * 1024 + lane * 16) = pack8(aq, 8, QSCALE);
        *(u32x4*)(ws + WS_K + ((size_t)hA * 64 + qt) * 1024 + lane * 16) = pack8(ak, 0, 1.0f);
        *(u32x4*)(ws + WS_K + ((size_t)(hA + 1) * 64 + qt) * 1024 + lane * 16) = pack8(ak, 8, 1.0f);
        {
            char* vb = ws + WS_V + (size_t)(hA + (r >> 4)) * VHEAD + qt * VTILE;
            *(u32x4*)(vb + h * 256 + (r & 15) * 16) = pack8(av, 0, 1.0f);
            *(u32x4*)(vb + 512 + h * 256 + (r & 15) * 16) = pack8(av, 8, 1.0f);
            if ((lane & 47) == 0) { const u32x4 ones = {0x3F803F80u, 0x3F803F80u, 0x3F803F80u, 0x3F803F80u}; *(u32x4*)(vb + 1024) = ones; }
        }
        float nA = 0.f, nB = 0.f;
#pragma unroll
        for (int j = 0; j < 8; ++j) { nA = fmaf(ak[j], ak[j], nA); nB = fmaf(ak[8 + j], ak[8 + j], nB); }
        nA = wave_max(half_sum(nA)); nB = wave_max(half_sum(nB));
        if (lane == 0) { float* km = (float*)(ws + WS_KMAX); km[hA * 64 + qt] = nA; km[(hA + 1) * 64 + qt] = nB; }
    } else {
        const int sw = w - 4, c = sw >> 1, ch = 2 * b + c, n = (sw & 1) * 64 + lane;
        LDSP char* buf = sm + K1_SSM + sw * K1_BUF;
        LDSP float* tab = (LDSP float*)(sm + K1_TAB);
        const float f_logA = bf2f(logA[ch * 128 + n]), f_aim = bf2f(aim[ch * 128 + n]);
        const float f_cre = bf2f(cre[ch * 128 + n]), f_cim = bf2f(cim[ch * 128 + n]), f_ldt = bf2f(logdt[ch]);
        for (int i = threadIdx.x - 256; i < 2 * 644; i += 256) tab[i] = 0.f;
        const float dt = bf16r(expf(f_ldt));
        const float xl = -bf16r(expf(f_logA)) * dt * 1.4426950408889634f;
        const float yr = f_aim * dt * 0.15915494309189535f;
        cplx C; C.r = 2.0f * (f_cre * dt); C.i = 2.0f * (f_cim * dt);
        const float mx = wave_max(fmaxf(fabsf(C.r), fabsf(C.i)));
        const int ex = __builtin_amdgcn_frexp_expf(mx);
        C.r = ldexpf(C.r, -ex); C.i = ldexpf(C.i, -ex);
        const cplx z1 = zpow(xl, yr, 1.f), z8 = zpow(xl, yr, 8.f), z256 = zpow(xl, yr, 256.f), z2 = cmul(z1, z1), z4 = cmul(z2, z2);
        BAR_LGKM();
        cplx v; v.r = 1.f; v.i = 0.f;
        LDSP unsigned* row = (LDSP unsigned*)(buf + lane * 4);
#pragma unroll
        for (int i = 0; i < 32; ++i) {
            row[i * (K1_ROW / 4)] = cvtpk_f16(v.r, v.i);
            v = cmul(v, z8);
            if (i == 10 || i == 21) BAR_LGKM();
        }
        f16x8 bfr[8];
#pragma unroll
        for (int t = 0; t < 8; ++t) bfr[t] = *(const LDSP f16x8*)(buf + r * K1_ROW + t * 32 + h * 16);
        BAR_LGKM();
        {
            cplx a = C;
#pragma unroll
            for (int i = 0; i < 8; ++i) { row[i * (K1_ROW / 4)] = cvtpk_f16(a.r, -a.i); a = cmul(a, z1); }
            cplx gk = cmul(C, z256); a = gk;
#pragma unroll
            for (int i = 0; i < 4; ++i) { row[(8 + i) * (K1_ROW / 4)] = cvtpk_f16(a.r, -a.i); a = cmul(a, z2); }
            gk = cmul(gk, z256); row[12 * (K1_ROW / 4)] = cvtpk_f16(gk.r, -gk.i); a = cmul(gk, z4); row[13 * (K1_ROW / 4)] = cvtpk_f16(a.r, -a.i);
            gk = cmul(gk, z256); row[14 * (K1_ROW / 4)] = cvtpk_f16(gk.r, -gk.i); a = cmul(gk, z4); row[15 * (K1_ROW / 4)] = cvtpk_f16(a.r, -a.i);
#pragma unroll
            for (int i = 0; i < 5; ++i) { gk = cmul(gk, z256); row[(16 + i) * (K1_ROW / 4)] = cvtpk_f16(gk.r, -gk.i); }
        }
        BAR_LGKM();
        f32x16 d = {};
#pragma unroll
        for (int t = 0; t < 8; ++t) d = mfma_f16(*(const LDSP f16x8*)(buf + r * K1_ROW + t * 32 + h * 16), bfr[t], d);
        {
            LDSP float* tc = tab + c * 644;
#pragma unroll
            for (int e = 0; e < 4; ++e) {
                __hip_atomic_fetch_add(tc + 8 * r + 4 * h + e, ldexpf(d[e], ex), __ATOMIC_RELAXED, __HIP_MEMORY_SCOPE_WORKGROUP);
                const int i1 = h ? 384 + 64 * (e >> 1) + 2 * r + (e & 1) : 256 + 4 * r + e;
                __hip_atomic_fetch_add(tc + i1, ldexpf(d[4 + e], ex), __ATOMIC_RELAXED, __HIP_MEMORY_SCOPE_WORKGROUP);
                if (h == 0) __hip_atomic_fetch_add(tc + 512 + 32 * e + r, ldexpf(d[8 + e], ex), __ATOMIC_RELAXED, __HIP_MEMORY_SCOPE_WORKGROUP);
            }
            if (lane == 32) __hip_atomic_fetch_add(tc + 640, ldexpf(d[8], ex), __ATOMIC_RELAXED, __HIP_MEMORY_SCOPE_WORKGROUP);
        }
        BAR_LGKM();
        {
            const int t = threadIdx.x - 256, j0 = 8 * t;
            int ci[9];
#pragma unroll
            for (int e = 0; e < 9; ++e) { const int j = j0 - 1 + e; const int bits = (int)(cvtpk_bf16((float)j, 0.f) & 0xffffu); ci[e] = j < 128 ? (j < 0 ? 0 : j) : bits - 17024; }
            char* img = ws + WS_TAPS + (size_t)b * TAP_IMG;
#pragma unroll
            for (int cc = 0; cc < 2; ++cc) {
                float k[9];
#pragma unroll
                for (int e = 0; e < 9; ++e) k[e] = tab[cc * 644 + ci[e]];
                if (t == 0) k[0] = 0.f;
                const u32x4 c0 = {cvtpk_bf16(k[1], k[2]), cvtpk_bf16(k[3], k[4]), cvtpk_bf16(k[5], k[6]), cvtpk_bf16(k[7], k[8])};
                const u32x4 c1 = {cvtpk_bf16(k[0], k[1]), cvtpk_bf16(k[2], k[3]), cvtpk_bf16(k[4], k[5]), cvtpk_bf16(k[6], k[7])};
                *(u32x4*)(img + cc * TAP_CH + (t + 8) * 16) = c0;
                *(u32x4*)(img + cc * TAP_CH + TAP_C1 + (t + 8) * 16) = c1;
            }
            if (t < 32) { const u32x4 z = {0, 0, 0, 0}; *(u32x4*)(img + (t >> 4) * TAP_CH + ((t >> 3) & 1) * TAP_C1 + (t & 7) * 16) = z; }
        }
        BAR_LGKM();
        BAR_LGKM();
    }
}

constexpr unsigned K2_K = 0, K2_V = 65536, K2_TAP = K2_V + VHEAD;
constexpr unsigned K2_UBLK = 144;
constexpr unsigned K2_U1 = K2_TAP + TAP_USED;
constexpr unsigned K2_Z = K2_U1 + 32 * K2_UBLK;
constexpr unsigned K2_U0 = K2_Z + 31 * K2_UBLK;
constexpr unsigned K2_LDS = K2_U0 + 32 * K2_UBLK;
static_assert(K2_LDS <= 163840, "LDS");

struct AttnState { f32x16 o; };

DI void attn_tiles(const LDSP char* kp, const LDSP char* vp0, const LDSP char* vp1, bf16x8 q, const f32x16& negm, f32x16& o, int t0, int t1) {
    f32x16 s = mfma_bf16(*(const LDSP bf16x8*)(kp + t0 * 1024), q, negm);
    for (int t = t0; t < t1; ++t) {
        const int tn = (t + 1 < t1) ? t + 1 : t;
        const bf16x8 kn = *(const LDSP bf16x8*)(kp + tn * 1024);
        const bf16x8 v0 = *(const LDSP bf16x8*)(vp0 + t * VTILE);
        const bf16x8 v1 = *(const LDSP bf16x8*)(vp1 + t * VTILE);
        const f32x16 sn = mfma_bf16(kn, q, negm);
        u32x4 p0, p1;
#pragma unroll
        for (int i = 0; i < 4; ++i) p0[i] = cvtpk_bf16(__builtin_amdgcn_exp2f(s[2 * i]), __builtin_amdgcn_exp2f(s[2 * i + 1]));
        o = mfma_bf16(v0, as_bf(p0), o);
#pragma unroll
        for (int i = 0; i < 4; ++i) p1[i] = cvtpk_bf16(__builtin_amdgcn_exp2f(s[8 + 2 * i]), __builtin_amdgcn_exp2f(s[9 + 2 * i]));
        o = mfma_bf16(v1, as_bf(p1), o);
        s = sn;
    }
}

__global__ __launch_bounds__(768) void k2_attn_conv(const unsigned short* __restrict__ u, const unsigned short* __restrict__ dskip,
                                                    char* __restrict__ ws, char* __restrict__ wso, unsigned short* __restrict__ out) {
    __shared__ __attribute__((aligned(16))) char smem[K2_LDS];
    LDSP char* const sm = (LDSP char*)smem;
    const unsigned lds0 = (unsigned)(uintptr_t)sm;
    const int lane = threadIdx.x & 63, w = __builtin_amdgcn_readfirstlane(threadIdx.x >> 6), b = blockIdx.x;
    const int r = lane & 31, h = lane >> 5;
    if (w < 8) {
        const int head = (b & 7) + 8 * ((b >> 3) & 3), qt = (b >> 5) * 8 + w;
        const char* kg = ws + WS_K + (size_t)head * 65536;
        const char* vg = ws + WS_V + (size_t)head * VHEAD;
        const unsigned voff = lane * 16;
        u32x4 qraw; float kmx;
        {
            const char* qg = ws + WS_Q + ((size_t)head * 64 + qt) * 1024;
            const char* mg = ws + WS_KMAX + (size_t)head * 256;
            asm volatile("global_load_dwordx4 %0, %1, %2" : "=&v"(qraw) : "v"(voff), "s"(qg) : "memory");
            asm volatile("global_load_dword %0, %1, %2" : "=&v"(kmx) : "v"(lane * 4), "s"(mg) : "memory");
        }
        if (w == 0) glds16(vg + 8 * 1024, voff, lds0 + K2_V + 8 * 1024);
        glds16(kg + w * 1024, voff, lds0 + K2_K + w * 1024);
        glds16(vg + w * 1024, voff, lds0 + K2_V + w * 1024);
#pragma unroll
        for (int i = 0; i < 7; ++i) {
            glds16(kg + (8 + 8 * i + w) * 1024, voff, lds0 + K2_K + (8 + 8 * i + w) * 1024);
            glds16(vg + (9 + 8 * i + w) * 1024, voff, lds0 + K2_V + (9 + 8 * i + w) * 1024);
        }
        asm volatile("s_waitcnt vmcnt(14)\n\ts_barrier" : "+v"(qraw), "+v"(kmx) :: "memory");
        const bf16x8 q = as_bf(qraw);
        float qn = 0.f;
#pragma unroll
        for (int i = 0; i < 4; ++i) { const float lo = __uint_as_float(qraw[i] << 16), hi = __uint_as_float(qraw[i] & 0xffff0000u); qn = fmaf(lo, lo, fmaf(hi, hi, qn)); }
        const float bound = __builtin_amdgcn_sqrtf(half_sum(qn) * wave_max(kmx)) * 1.02f;
        const bool safe = __all(bound <= SAFE_BOUND);
        const LDSP char* kp = sm + K2_K + lane * 16;
        const LDSP char* vp0 = sm + K2_V + (r < 16 ? h * 256 + r * 16 : 1024);
        const LDSP char* vp1 = sm + K2_V + (r < 16 ? 512 + h * 256 + r * 16 : 1024);
        f32x16 o = {};
        f32x16 negm;
#pragma unroll
        for (int i = 0; i < 16; ++i) negm[i] = -bound;
        if (safe) attn_tiles(kp, vp0, vp1, q, negm, o, 0, 8);
        WAIT_BAR(0);
        int t0 = 8;
        if (!safe) {
            float m = -INFINITY;
            const f32x16 zero = {};
            for (int t = 0; t < 64; ++t) {
                const f32x16 s = mfma_bf16(*(const LDSP bf16x8*)(kp + t * 1024), q, zero);
#pragma unroll
                for (int i = 0; i < 16; ++i) m = fmaxf(m, s[i]);
            }
            m = half_max(m);
#pragma unroll
            for (int i = 0; i < 16; ++i) negm[i] = -m;
            t0 = 0;
        }
        attn_tiles(kp, vp0, vp1, q, negm, o, t0, 64);
        const float rl = 1.0f / o[8];
        *(u32x4*)(wso + ((size_t)qt * 32 + head) * 1024 + lane * 16) = pack8(o, 0, rl);
    } else {
        const int hw = w - 8, c = hw >> 1, rho = hw & 1, cp = 32 * (b & 7) + (b >> 3), ch = 2 * cp + c;
        const char* tg = ws + WS_TAPS + (size_t)cp * TAP_IMG;
#pragma unroll
        for (int i = 0; i < 4; ++i) glds16(tg + (hw + 4 * i) * 1024, lane * 16, lds0 + K2_TAP + (hw + 4 * i) * 1024);
        if (hw == 0 && lane < (TAP_USED - 16384) / 16) glds16(tg + 16384, lane * 16, lds0 + K2_TAP + 16384);
        unsigned ur[8];
#pragma unroll
        for (int i = 0; i < 8; ++i) ur[i] = *(const unsigned*)((const char*)u + (size_t)(512 * hw + 64 * i + lane) * 1024 + cp * 4);
        const float dsk = bf2f(dskip[ch]);
        { const u32x4 z = {0, 0, 0, 0};
          for (int i = threadIdx.x - 512; i < 31 * 9; i += 256) *(LDSP u32x4*)(sm + K2_Z + i * 16) = z; }
        asm volatile("s_barrier" ::: "memory");
        {
            const unsigned pos = 2 * (lane ^ 7);
#pragma unroll
            for (int i = 0; i < 8; ++i) {
                const int blk = 8 * hw + i;
                *(LDSP unsigned short*)(sm + K2_U0 + blk * K2_UBLK + pos) = (unsigned short)(ur[i] & 0xffffu);
                *(LDSP unsigned short*)(sm + K2_Z - (blk + 1) * K2_UBLK + pos) = (unsigned short)(ur[i] >> 16);
            }
        }
        WAIT_BAR(0);
        const int e0 = 32 * rho + r - 8 * h - 7 + 64;
        const LDSP char* ap = sm + K2_TAP + c * TAP_CH + ((r & 1) ? 2 * e0 : TAP_C1 + 2 * (e0 + 1)) - 96;
        const int ustep = c ? (int)K2_UBLK : -(int)K2_UBLK;
        const LDSP char* bp = sm + (c ? K2_Z - (r + 1) * K2_UBLK : K2_U0 + r * K2_UBLK) + 16 * h;
        f32x16 y = {};
        for (int m = 0; m < 32; ++m) {
#pragma unroll
            for (int k = 0; k < 4; ++k) {
                const u32x4a a = *(const LDSP u32x4a*)(ap + 96 - 32 * k);
                const bf16x8 bb = *(const LDSP bf16x8*)(bp + 32 * k);
                y = mfma_bf16(as_bf((u32x4)a), bb, y);
            }
            ap += 128; bp += ustep;
        }
        const LDSP char* up = sm + (c ? K2_Z - (r + 1) * K2_UBLK : K2_U0 + r * K2_UBLK) + 64 * rho + 8 * (1 - h);
        unsigned short* op = out + (size_t)(64 * r + 32 * rho + 4 * h) * 1024 + ch;
#pragma unroll
        for (int gq = 0; gq < 4; ++gq) {
            const u32x2 uu = *(const LDSP u32x2*)(up + 16 * gq);
#pragma unroll
            for (int e = 0; e < 4; ++e) {
                const unsigned pr = uu[(3 - e) >> 1];
                const float uf = __uint_as_float(((3 - e) & 1) ? (pr & 0xffff0000u) : (pr << 16));
                const float res = bf16r(y[4 * gq + e]) + bf16r(uf * dsk);
                op[(size_t)(8 * gq + e) * 1024] = (unsigned short)(cvtpk_bf16(res, res) & 0xffffu);
            }
        }
    }
}

constexpr unsigned K3_O = 0, K3_W = 65536, K3_LDS = 131072;

__global__ __launch_bounds__(256) void k3_oproj(const char* __restrict__ wso, const unsigned short* __restrict__ wo, unsigned short* __restrict__ out) {
    __shared__ __attribute__((aligned(16))) char smem[K3_LDS];
    LDSP char* const sm = (LDSP char*)smem;
    const unsigned lds0 = (unsigned)(uintptr_t)sm;
    const int lane = threadIdx.x & 63, w = __builtin_amdgcn_readfirstlane(threadIdx.x >> 6), b = blockIdx.x;
    const int r = lane & 31, h = lane >> 5;
    const int qp = (b & 7) * 4 + ((b >> 3) & 3), np = b >> 5;
    const char* og = wso + (size_t)qp * 65536;
    const char* wg = (const char*)wo + np * 128;
    const unsigned woff = (lane >> 2) * 1024 + (lane & 3) * 16;
#pragma unroll
    for (int ck = 0; ck < 4; ++ck) {
#pragma unroll
        for (int i = 0; i < 4; ++i) {
            const int blk = (i >> 1) * 32 + ck * 8 + (i & 1) * 4 + w;
            glds16(og + blk * 1024, lane * 16, lds0 + K3_O + blk * 1024);
            const int strip = i >> 1, kr = ck * 128 + ((i & 1) * 4 + w) * 16;
            glds16(wg + (size_t)kr * 1024 + strip * 64, woff, lds0 + K3_W + strip * 32768 + kr * 64);
        }
    }
    const int wq = w & 1, wn = w >> 1;
    const LDSP char* bp = sm + K3_O + wq * 32768 + lane * 16;
    const LDSP char* ap = sm + K3_W + wn * 32768 + (4 * h + ((lane & 15) >> 2)) * 64 + (((lane >> 4) & 1) * 16 + (lane & 3) * 4) * 2;
    f32x16 acc = {};
#pragma unroll
    for (int ck = 0; ck < 4; ++ck) {
        if (ck == 0) WAIT_BAR(24); else if (ck == 1) WAIT_BAR(16); else if (ck == 2) WAIT_BAR(8); else WAIT_BAR(0);
#pragma unroll
        for (int i = 0; i < 8; ++i) {
            const int hd = ck * 8 + i;
            const bf16x8 a = tr_frag(ap, hd * 1024, hd * 1024 + 512);
            const bf16x8 bb = *(const LDSP bf16x8*)(bp + hd * 1024);
            acc = mfma_bf16(a, bb, acc);
        }
    }
    char* op = (char*)out + (size_t)(qp * 64 + wq * 32 + r) * 2048 + 1024 + (np * 64 + wn * 32) * 2 + h * 16;
#pragma unroll
    for (int gp = 0; gp < 2; ++gp) {
        unsigned a0 = cvtpk_bf16(acc[8 * gp + 0], acc[8 * gp + 1]), a1 = cvtpk_bf16(acc[8 * gp + 2], acc[8 * gp + 3]);
        unsigned b0 = cvtpk_bf16(acc[8 * gp + 4], acc[8 * gp + 5]), b1 = cvtpk_bf16(acc[8 * gp + 6], acc[8 * gp + 7]);
        const auto s0 = __builtin_amdgcn_permlane32_swap(a0, b0, false, false);
        const auto s1 = __builtin_amdgcn_permlane32_swap(a1, b1, false, false);
        const u32x4 v = {s0[0], s1[0], s0[1], s1[1]};
        *(u32x4*)(op + gp * 32) = v;
    }
}

extern "C" void kernel_launch(void* const* d_in, const int* in_sizes, int n_in, void* d_out, int out_size, void* d_ws, size_t ws_size,
                              hipStream_t stream) {
    const unsigned short* u = (const unsigned short*)d_in[0];
    const unsigned short* logA = (const unsigned short*)d_in[1];
    const unsigned short* aim = (const unsigned short*)d_in[2];
    const unsigned short* cre = (const unsigned short*)d_in[3];
    const unsigned short* cim = (const unsigned short*)d_in[4];
    const unsigned short* logdt = (const unsigned short*)d_in[5];
    const unsigned short* dskip = (const unsigned short*)d_in[6];
    const unsigned short* x = (const unsigned short*)d_in[7];
    const unsigned short* wqkv = (const unsigned short*)d_in[8];
    const unsigned short* wo = (const unsigned short*)d_in[9];
    char* ws = (char*)d_ws;
    unsigned short* out = (unsigned short*)d_out;
    k1_qkv_taps<<<256, 512, 0, stream>>>(x, wqkv, logA, aim, cre, cim, logdt, ws);
    k2_attn_conv<<<256, 768, 0, stream>>>(u, dskip, ws, ws + WS_O, out);
    k3_oproj<<<256, 256, 0, stream>>>(ws + WS_O, wo, out);
}
```

```cpp
#include <hip/hip_runtime.h>
#include <hip/hip_bf16.h>
#include <stdint.h>

typedef __bf16 bf16x8 __attribute__((ext_vector_type(8)));
typedef __bf16 bf16x2 __attribute__((ext_vector_type(2)));
typedef _Float16 f16x8 __attribute__((ext_vector_type(8)));
typedef _Float16 f16x2 __attribute__((ext_vector_type(2)));
typedef float f32x16 __attribute__((ext_vector_type(16)));
typedef float f32x2 __attribute__((ext_vector_type(2)));
typedef unsigned u32x4 __attribute__((ext_vector_type(4)));
typedef unsigned u32x4a __attribute__((ext_vector_type(4), aligned(4)));
typedef unsigned u32x2 __attribute__((ext_vector_type(2)));
typedef short s16x4 __attribute__((ext_vector_type(4)));
#define LDSP __attribute__((address_space(3)))
#define DI __device__ __forceinline__

constexpr int SEQ = 2048, DM = 512, NH = 32;
constexpr unsigned VTILE = 1040;
constexpr unsigned VHEAD = 64 * VTILE;
constexpr unsigned TAP_C1 = 4288, TAP_CH = 8512, TAP_IMG = 17408, TAP_USED = 17024;
constexpr size_t WS_Q = 0;
constexpr size_t WS_K = WS_Q + (size_t)NH * 65536;
constexpr size_t WS_V = WS_K + (size_t)NH * 65536;
constexpr size_t WS_KMAX = WS_V + (size_t)NH * VHEAD;
constexpr size_t WS_TAPS = WS_KMAX + (size_t)NH * 64 * 4;
constexpr size_t WS_O = WS_TAPS + (size_t)256 * TAP_IMG;
constexpr float QSCALE = 0.25f * 1.4426950408889634f;
constexpr float SAFE_BOUND = 40.0f;

#define STAMP_DECL
#define STAMP(i)
#define STAMP_OUT(kern, base)
DI float bf2f(unsigned short v) { return __uint_as_float((unsigned)v << 16); }
DI unsigned cvtpk_bf16(float lo, float hi) { f32x2 v = {lo, hi}; return __builtin_bit_cast(unsigned, __builtin_convertvector(v, bf16x2)); }
DI unsigned cvtpk_f16(float lo, float hi) { f32x2 v = {lo, hi}; return __builtin_bit_cast(unsigned, __builtin_convertvector(v, f16x2)); }
DI float bf16r(float x) { return __uint_as_float(cvtpk_bf16(x, x) << 16); }
DI f32x16 mfma_bf16(bf16x8 a, bf16x8 b, f32x16 c) { return __builtin_amdgcn_mfma_f32_32x32x16_bf16(a, b, c, 0, 0, 0); }
DI f32x16 mfma_f16(f16x8 a, f16x8 b, f32x16 c) { return __builtin_amdgcn_mfma_f32_32x32x16_f16(a, b, c, 0, 0, 0); }
DI bf16x8 as_bf(u32x4 v) { return __builtin_bit_cast(bf16x8, v); }
DI void glds16(const void* sbase, unsigned voff, unsigned lds) {
    unsigned sv;
    asm volatile("s_mov_b32 %0, m0\n\ts_mov_b32 m0, %3\n\ts_nop 0\n\tglobal_load_lds_dwordx4 %1, %2\n\ts_mov_b32 m0, %0"
                 : "=&s"(sv) : "v"(voff), "s"(sbase), "s"(lds) : "memory");
}
#define WAIT_VM(n) asm volatile("s_waitcnt vmcnt(" #n ")" ::: "memory")
#define WAIT_BAR(n) asm volatile("s_waitcnt vmcnt(" #n ") lgkmcnt(0)\n\ts_barrier" ::: "memory")
#define BAR_LGKM() asm volatile("s_waitcnt lgkmcnt(0)\n\ts_barrier" ::: "memory")
template <int CTRL> DI float dpp_max(float v) {
    const int o = __builtin_amdgcn_update_dpp(__float_as_int(v), __float_as_int(v), CTRL, 0xf, 0xf, false);
    return fmaxf(v, __int_as_float(o));
}
DI float wave_max(float v) {
    v = dpp_max<0x111>(v); v = dpp_max<0x112>(v); v = dpp_max<0x114>(v); v = dpp_max<0x118>(v);
    v = dpp_max<0x142>(v); v = dpp_max<0x143>(v);
    return __int_as_float(__builtin_amdgcn_readlane(__float_as_int(v), 63));
}
DI float half_sum(float v) {
    auto r = __builtin_amdgcn_permlane32_swap(__float_as_uint(v), __float_as_uint(v), false, false);
    return __uint_as_float(r[0]) + __uint_as_float(r[1]);
}
DI float half_max(float v) {
    auto r = __builtin_amdgcn_permlane32_swap(__float_as_uint(v), __float_as_uint(v), false, false);
    return fmaxf(__uint_as_float(r[0]), __uint_as_float(r[1]));
}
DI u32x4 pack8(const f32x16& a, int o, float s) {
    u32x4 r;
    r[0] = cvtpk_bf16(a[o + 0] * s, a[o + 1] * s); r[1] = cvtpk_bf16(a[o + 2] * s, a[o + 3] * s);
    r[2] = cvtpk_bf16(a[o + 4] * s, a[o + 5] * s); r[3] = cvtpk_bf16(a[o + 6] * s, a[o + 7] * s);
    return r;
}
DI bf16x8 tr_frag(const LDSP char* p, int off_lo, int off_hi) {
    const s16x4 lo = __builtin_amdgcn_ds_read_tr16_b64_v4i16((LDSP s16x4*)(p + off_lo));
    const s16x4 hi = __builtin_amdgcn_ds_read_tr16_b64_v4i16((LDSP s16x4*)(p + off_hi));
    typedef short s16x8 __attribute__((ext_vector_type(8)));
    const s16x8 v = __builtin_shufflevector(lo, hi, 0, 1, 2, 3, 4, 5, 6, 7);
    return __builtin_bit_cast(bf16x8, v);
}

constexpr unsigned K1_STAGE = 28672, K1_W = 16384;
constexpr unsigned K1_SSM = 4 * K1_STAGE;
constexpr unsigned K1_ROW = 272, K1_BUF = 32 * K1_ROW;
constexpr unsigned K1_TAB = K1_SSM + 4 * K1_BUF;
constexpr unsigned K1_LDS = K1_TAB + 2 * 644 * 4;

struct cplx { float r, i; };
DI cplx cmul(cplx a, cplx b) { cplx o; o.r = a.r * b.r - a.i * b.i; o.i = a.r * b.i + a.i * b.r; return o; }
DI cplx zpow(float xl, float yr, float p) {
    const float e = __builtin_amdgcn_exp2f(xl * p);
    const float th = __builtin_amdgcn_fractf(yr * p);
    cplx o; o.r = e * __builtin_amdgcn_cosf(th); o.i = e * __builtin_amdgcn_sinf(th); return o;
}

__global__ __launch_bounds__(512) void k1_qkv_taps(const unsigned short* __restrict__ x, const unsigned short* __restrict__ wqkv,
                                                   const unsigned short* __restrict__ logA, const unsigned short* __restrict__ aim,
                                                   const unsigned short* __restrict__ cre, const unsigned short* __restrict__ cim,
                                                   const unsigned short* __restrict__ logdt, char* __restrict__ ws) {
    __shared__ __attribute__((aligned(16))) char smem[K1_LDS];
    LDSP char* const sm = (LDSP char*)smem;
    const unsigned lds0 = (unsigned)(uintptr_t)sm;
    const int lane = threadIdx.x & 63, w = __builtin_amdgcn_readfirstlane(threadIdx.x >> 6), b = blockIdx.x;
    const int r = lane & 31, h = lane >> 5;
    STAMP_DECL; STAMP(0);
    if (w < 4) {
        const int g = b & 7, idx = b >> 3, tb = (g & 3) * 4 + (idx & 3), hp = (g >> 2) * 8 + (idx >> 2);
        unsigned xoff[4];
#pragma unroll
        for (int i = 0; i < 4; ++i) { const int rr = 8 * i + (lane >> 3); xoff[i] = rr * 1024 + (((lane & 7) ^ ((rr >> 1) & 7)) << 4); }
        const unsigned woff = (lane >> 2) * 3072 + (lane & 3) * 16;
        const char* xb = (const char*)x + (size_t)(tb * 128 + w * 32) * 1024;
        const char* wb = (const char*)wqkv + (size_t)(w * 16) * 3072 + hp * 64;
        auto issue = [&](int s) {
            const unsigned slot = lds0 + (s & 3) * K1_STAGE;
#pragma unroll
            for (int i = 0; i < 4; ++i) glds16(xb + s * 128, xoff[i], slot + w * 4096 + i * 1024);
#pragma unroll
            for (int j = 0; j < 3; ++j) glds16(wb + (size_t)s * 64 * 3072 + j * 1024, woff, slot + K1_W + j * 4096 + w * 1024);
        };
        issue(0); issue(1); issue(2);
        STAMP(1);
        unsigned xa[4];
#pragma unroll
        for (int t = 0; t < 4; ++t) xa[t] = (w * 32 + r) * 128 + (((2 * t + h) ^ ((r >> 1) & 7)) << 4);
        const unsigned wa = K1_W + (8 * h + ((lane & 15) >> 2)) * 64 + (((lane >> 4) & 1) * 16 + (lane & 3) * 4) * 2;
        f32x16 aq = {}, ak = {}, av = {};
#pragma unroll
        for (int s = 0; s < 8; ++s) {
            if (s <= 5) WAIT_BAR(14); else if (s == 6) WAIT_BAR(7); else WAIT_BAR(0);
            if (s == 0) STAMP(2);
            if (s == 4) STAMP(3);
            if (s + 3 < 8) issue(s + 3);
            const LDSP char* sb = sm + (s & 3) * K1_STAGE;
#pragma unroll
            for (int t = 0; t < 4; ++t) {
                const bf16x8 xf = *(const LDSP bf16x8*)(sb + xa[t]);
                const bf16x8 wq = tr_frag(sb + wa, t * 1024, t * 1024 + 256);
                const bf16x8 wk = tr_frag(sb + wa, 4096 + t * 1024, 4096 + t * 1024 + 256);
                const bf16x8 wv = tr_frag(sb + wa, 8192 + t * 1024, 8192 + t * 1024 + 256);
                aq = mfma_bf16(wq, xf, aq);
                ak = mfma_bf16(wk, xf, ak);
                av = mfma_bf16(xf, wv, av);
            }
        }
        STAMP(4);
        const int qt = tb * 4 + w, hA = 2 * hp;
        *(u32x4*)(ws + WS_Q + ((size_t)hA * 64 + qt) * 1024 + lane * 16) = pack8(aq, 0, QSCALE);
        *(u32x4*)(ws + WS_Q + ((size_t)(hA + 1) * 64 + qt) * 1024 + lane * 16) = pack8(aq, 8, QSCALE);
        *(u32x4*)(ws + WS_K + ((size_t)hA * 64 + qt) * 1024 + lane * 16) = pack8(ak, 0, 1.0f);
        *(u32x4*)(ws + WS_K + ((size_t)(hA + 1) * 64 + qt) * 1024 + lane * 16) = pack8(ak, 8, 1.0f);
        {
            char* vb = ws + WS_V + (size_t)(hA + (r >> 4)) * VHEAD + qt * VTILE;
            *(u32x4*)(vb + h * 256 + (r & 15) * 16) = pack8(av, 0, 1.0f);
            *(u32x4*)(vb + 512 + h * 256 + (r & 15) * 16) = pack8(av, 8, 1.0f);
            if ((lane & 47) == 0) { const u32x4 ones = {0x3F803F80u, 0x3F803F80u, 0x3F803F80u, 0x3F803F80u}; *(u32x4*)(vb + 1024) = ones; }
        }
        float nA = 0.f, nB = 0.f;
#pragma unroll
        for (int j = 0; j < 8; ++j) { nA = fmaf(ak[j], ak[j], nA); nB = fmaf(ak[8 + j], ak[8 + j], nB); }
        nA = wave_max(half_sum(nA)); nB = wave_max(half_sum(nB));
        if (lane == 0) { float* km = (float*)(ws + WS_KMAX); km[hA * 64 + qt] = nA; km[(hA + 1) * 64 + qt] = nB; }
        STAMP(5);
    } else {
        const int sw = w - 4, c = sw >> 1, ch = 2 * b + c, n = (sw & 1) * 64 + lane;
        LDSP char* buf = sm + K1_SSM + sw * K1_BUF;
        LDSP float* tab = (LDSP float*)(sm + K1_TAB);
        const float f_logA = bf2f(logA[ch * 128 + n]), f_aim = bf2f(aim[ch * 128 + n]);
        const float f_cre = bf2f(cre[ch * 128 + n]), f_cim = bf2f(cim[ch * 128 + n]), f_ldt = bf2f(logdt[ch]);
        for (int i = threadIdx.x - 256; i < 2 * 644; i += 256) tab[i] = 0.f;
        const float dt = bf16r(expf(f_ldt));
        const float xl = -bf16r(expf(f_logA)) * dt * 1.4426950408889634f;
        const float yr = f_aim * dt * 0.15915494309189535f;
        cplx C; C.r = 2.0f * (f_cre * dt); C.i = 2.0f * (f_cim * dt);
        const float mx = wave_max(fmaxf(fabsf(C.r), fabsf(C.i)));
        const int ex = __builtin_amdgcn_frexp_expf(mx);
        C.r = ldexpf(C.r, -ex); C.i = ldexpf(C.i, -ex);
        const cplx z1 = zpow(xl, yr, 1.f), z8 = zpow(xl, yr, 8.f), z256 = zpow(xl, yr, 256.f), z2 = cmul(z1, z1), z4 = cmul(z2, z2);
        STAMP(1);
        BAR_LGKM();
        STAMP(2);
        cplx v; v.r = 1.f; v.i = 0.f;
        LDSP unsigned* row = (LDSP unsigned*)(buf + lane * 4);
#pragma unroll
        for (int i = 0; i < 32; ++i) {
            row[i * (K1_ROW / 4)] = cvtpk_f16(v.r, v.i);
            v = cmul(v, z8);
            if (i == 10 || i == 21) BAR_LGKM();
        }
        f16x8 bfr[8];
#pragma unroll
        for (int t = 0; t < 8; ++t) bfr[t] = *(const LDSP f16x8*)(buf + r * K1_ROW + t * 32 + h * 16);
        STAMP(3);
        BAR_LGKM();
        {
            cplx a = C;
#pragma unroll
            for (int i = 0; i < 8; ++i) { row[i * (K1_ROW / 4)] = cvtpk_f16(a.r, -a.i); a = cmul(a, z1); }
            cplx gk = cmul(C, z256); a = gk;
#pragma unroll
            for (int i = 0; i < 4; ++i) { row[(8 + i) * (K1_ROW / 4)] = cvtpk_f16(a.r, -a.i); a = cmul(a, z2); }
            gk = cmul(gk, z256); row[12 * (K1_ROW / 4)] = cvtpk_f16(gk.r, -gk.i); a = cmul(gk, z4); row[13 * (K1_ROW / 4)] = cvtpk_f16(a.r, -a.i);
            gk = cmul(gk, z256); row[14 * (K1_ROW / 4)] = cvtpk_f16(gk.r, -gk.i); a = cmul(gk, z4); row[15 * (K1_ROW / 4)] = cvtpk_f16(a.r, -a.i);
#pragma unroll
            for (int i = 0; i < 5; ++i) { gk = cmul(gk, z256); row[(16 + i) * (K1_ROW / 4)] = cvtpk_f16(gk.r, -gk.i); }
        }
        STAMP(4);
        BAR_LGKM();
        f32x16 d = {};
#pragma unroll
        for (int t = 0; t < 8; ++t) d = mfma_f16(*(const LDSP f16x8*)(buf + r * K1_ROW + t * 32 + h * 16), bfr[t], d);
        {
            LDSP float* tc = tab + c * 644;
#pragma unroll
            for (int e = 0; e < 4; ++e) {
                __hip_atomic_fetch_add(tc + 8 * r + 4 * h + e, ldexpf(d[e], ex), __ATOMIC_RELAXED, __HIP_MEMORY_SCOPE_WORKGROUP);
                const int i1 = h ? 384 + 64 * (e >> 1) + 2 * r + (e & 1) : 256 + 4 * r + e;
                __hip_atomic_fetch_add(tc + i1, ldexpf(d[4 + e], ex), __ATOMIC_RELAXED, __HIP_MEMORY_SCOPE_WORKGROUP);
                if (h == 0) __hip_atomic_fetch_add(tc + 512 + 32 * e + r, ldexpf(d[8 + e], ex), __ATOMIC_RELAXED, __HIP_MEMORY_SCOPE_WORKGROUP);
            }
            if (lane == 32) __hip_atomic_fetch_add(tc + 640, ldexpf(d[8], ex), __ATOMIC_RELAXED, __HIP_MEMORY_SCOPE_WORKGROUP);
        }
        STAMP(5);
        BAR_LGKM();
        {
            const int t = threadIdx.x - 256, j0 = 8 * t;
            int ci[9];
#pragma unroll
            for (int e = 0; e < 9; ++e) { const int j = j0 - 1 + e; const int bits = (int)(cvtpk_bf16((float)j, 0.f) & 0xffffu); ci[e] = j < 128 ? (j < 0 ? 0 : j) : bits - 17024; }
            char* img = ws + WS_TAPS + (size_t)b * TAP_IMG;
#pragma unroll
            for (int cc = 0; cc < 2; ++cc) {
                float k[9];
#pragma unroll
                for (int e = 0; e < 9; ++e) k[e] = tab[cc * 644 + ci[e]];
                if (t == 0) k[0] = 0.f;
                const u32x4 c0 = {cvtpk_bf16(k[1], k[2]), cvtpk_bf16(k[3], k[4]), cvtpk_bf16(k[5], k[6]), cvtpk_bf16(k[7], k[8])};
                const u32x4 c1 = {cvtpk_bf16(k[0], k[1]), cvtpk_bf16(k[2], k[3]), cvtpk_bf16(k[4], k[5]), cvtpk_bf16(k[6], k[7])};
                *(u32x4*)(img + cc * TAP_CH + (t + 8) * 16) = c0;
                *(u32x4*)(img + cc * TAP_CH + TAP_C1 + (t + 8) * 16) = c1;
            }
            if (t < 32) { const u32x4 z = {0, 0, 0, 0}; *(u32x4*)(img + (t >> 4) * TAP_CH + ((t >> 3) & 1) * TAP_C1 + (t & 7) * 16) = z; }
        }
        STAMP(6);
        BAR_LGKM();
        BAR_LGKM();
        STAMP(7);
    }
    STAMP_OUT(0, ws);
}

constexpr unsigned K2_K = 0, K2_V = 65536, K2_TAP = K2_V + VHEAD;
constexpr unsigned K2_UBLK = 144;
constexpr unsigned K2_U1 = K2_TAP + TAP_USED;
constexpr unsigned K2_Z = K2_U1 + 32 * K2_UBLK;
constexpr unsigned K2_U0 = K2_Z + 31 * K2_UBLK;
constexpr unsigned K2_LDS = K2_U0 + 32 * K2_UBLK;
static_assert(K2_LDS <= 163840, "LDS");

struct AttnState { f32x16 o; };

DI void attn_tiles(const LDSP char* kp, const LDSP char* vp0, const LDSP char* vp1, bf16x8 q, const f32x16& negm, f32x16& o, int t0, int t1) {
    f32x16 s = mfma_bf16(*(const LDSP bf16x8*)(kp + t0 * 1024), q, negm);
    for (int t = t0; t < t1; ++t) {
        const int tn = (t + 1 < t1) ? t + 1 : t;
        const bf16x8 kn = *(const LDSP bf16x8*)(kp + tn * 1024);
        const bf16x8 v0 = *(const LDSP bf16x8*)(vp0 + t * VTILE);
        const bf16x8 v1 = *(const LDSP bf16x8*)(vp1 + t * VTILE);
        const f32x16 sn = mfma_bf16(kn, q, negm);
        u32x4 p0, p1;
#pragma unroll
        for (int i = 0; i < 4; ++i) p0[i] = cvtpk_bf16(__builtin_amdgcn_exp2f(s[2 * i]), __builtin_amdgcn_exp2f(s[2 * i + 1]));
        o = mfma_bf16(v0, as_bf(p0), o);
#pragma unroll
        for (int i = 0; i < 4; ++i) p1[i] = cvtpk_bf16(__builtin_amdgcn_exp2f(s[8 + 2 * i]), __builtin_amdgcn_exp2f(s[9 + 2 * i]));
        o = mfma_bf16(v1, as_bf(p1), o);
        s = sn;
    }
}

__global__ __launch_bounds__(768) void k2_attn_conv(const unsigned short* __restrict__ u, const unsigned short* __restrict__ dskip,
                                                    char* __restrict__ ws, char* __restrict__ wso, unsigned short* __restrict__ out) {
    __shared__ __attribute__((aligned(16))) char smem[K2_LDS];
    LDSP char* const sm = (LDSP char*)smem;
    const unsigned lds0 = (unsigned)(uintptr_t)sm;
    const int lane = threadIdx.x & 63, w = __builtin_amdgcn_readfirstlane(threadIdx.x >> 6), b = blockIdx.x;
    const int r = lane & 31, h = lane >> 5;
    STAMP_DECL; STAMP(0);
    if (w < 8) {
        const int head = (b & 7) + 8 * ((b >> 3) & 3), qt = (b >> 5) * 8 + w;
        const char* kg = ws + WS_K + (size_t)head * 65536;
        const char* vg = ws + WS_V + (size_t)head * VHEAD;
        const unsigned voff = lane * 16;
        u32x4 qraw; float kmx;
        {
            const char* qg = ws + WS_Q + ((size_t)head * 64 + qt) * 1024;
            const char* mg = ws + WS_KMAX + (size_t)head * 256;
            asm volatile("global_load_dwordx4 %0, %1, %2" : "=&v"(qraw) : "v"(voff), "s"(qg) : "memory");
            asm volatile("global_load_dword %0, %1, %2" : "=&v"(kmx) : "v"(lane * 4), "s"(mg) : "memory");
        }
        if (w == 0) glds16(vg + 8 * 1024, voff, lds0 + K2_V + 8 * 1024);
        glds16(kg + w * 1024, voff, lds0 + K2_K + w * 1024);
        glds16(vg + w * 1024, voff, lds0 + K2_V + w * 1024);
#pragma unroll
        for (int i = 0; i < 7; ++i) {
            glds16(kg + (8 + 8 * i + w) * 1024, voff, lds0 + K2_K + (8 + 8 * i + w) * 1024);
            glds16(vg + (9 + 8 * i + w) * 1024, voff, lds0 + K2_V + (9 + 8 * i + w) * 1024);
        }
        STAMP(1);
        asm volatile("s_waitcnt vmcnt(14)\n\ts_barrier" : "+v"(qraw), "+v"(kmx) :: "memory");
        STAMP(2);
        const bf16x8 q = as_bf(qraw);
        float qn = 0.f;
#pragma unroll
        for (int i = 0; i < 4; ++i) { const float lo = __uint_as_float(qraw[i] << 16), hi = __uint_as_float(qraw[i] & 0xffff0000u); qn = fmaf(lo, lo, fmaf(hi, hi, qn)); }
        const float bound = __builtin_amdgcn_sqrtf(half_sum(qn) * wave_max(kmx)) * 1.02f;
        const bool safe = __all(bound <= SAFE_BOUND);
        const LDSP char* kp = sm + K2_K + lane * 16;
        const LDSP char* vp0 = sm + K2_V + (r < 16 ? h * 256 + r * 16 : 1024);
        const LDSP char* vp1 = sm + K2_V + (r < 16 ? 512 + h * 256 + r * 16 : 1024);
        f32x16 o = {};
        f32x16 negm;
#pragma unroll
        for (int i = 0; i < 16; ++i) negm[i] = -bound;
        if (safe) attn_tiles(kp, vp0, vp1, q, negm, o, 0, 8);
        STAMP(3);
        WAIT_BAR(0);
        STAMP(4);
        int t0 = 8;
        if (!safe) {
            float m = -INFINITY;
            const f32x16 zero = {};
            for (int t = 0; t < 64; ++t) {
                const f32x16 s = mfma_bf16(*(const LDSP bf16x8*)(kp + t * 1024), q, zero);
#pragma unroll
                for (int i = 0; i < 16; ++i) m = fmaxf(m, s[i]);
            }
            m = half_max(m);
#pragma unroll
            for (int i = 0; i < 16; ++i) negm[i] = -m;
            t0 = 0;
        }
        attn_tiles(kp, vp0, vp1, q, negm, o, t0, 64);
        STAMP(5);
        const float rl = 1.0f / o[8];
        *(u32x4*)(wso + ((size_t)qt * 32 + head) * 1024 + lane * 16) = pack8(o, 0, rl);
        STAMP(6);
    } else {
        const int hw = w - 8, c = hw >> 1, rho = hw & 1, cp = 32 * (b & 7) + (b >> 3), ch = 2 * cp + c;
        const char* tg = ws + WS_TAPS + (size_t)cp * TAP_IMG;
#pragma unroll
        for (int i = 0; i < 4; ++i) glds16(tg + (hw + 4 * i) * 1024, lane * 16, lds0 + K2_TAP + (hw + 4 * i) * 1024);
        if (hw == 0 && lane < (TAP_USED - 16384) / 16) glds16(tg + 16384, lane * 16, lds0 + K2_TAP + 16384);
        unsigned ur[8];
#pragma unroll
        for (int i = 0; i < 8; ++i) ur[i] = *(const unsigned*)((const char*)u + (size_t)(512 * hw + 64 * i + lane) * 1024 + cp * 4);
        const float dsk = bf2f(dskip[ch]);
        { const u32x4 z = {0, 0, 0, 0};
          for (int i = threadIdx.x - 512; i < 31 * 9; i += 256) *(LDSP u32x4*)(sm + K2_Z + i * 16) = z; }
        STAMP(1);
        asm volatile("s_barrier" ::: "memory");
        STAMP(2);
        {
            const unsigned pos = 2 * (lane ^ 7);
#pragma unroll
            for (int i = 0; i < 8; ++i) {
                const int blk = 8 * hw + i;
                *(LDSP unsigned short*)(sm + K2_U0 + blk * K2_UBLK + pos) = (unsigned short)(ur[i] & 0xffffu);
                *(LDSP unsigned short*)(sm + K2_Z - (blk + 1) * K2_UBLK + pos) = (unsigned short)(ur[i] >> 16);
            }
        }
        STAMP(3);
        WAIT_BAR(0);
        STAMP(4);
        const int e0 = 32 * rho + r - 8 * h - 7 + 64;
        const LDSP char* ap = sm + K2_TAP + c * TAP_CH + ((r & 1) ? 2 * e0 : TAP_C1 + 2 * (e0 + 1)) - 96;
        const int ustep = c ? (int)K2_UBLK : -(int)K2_UBLK;
        const LDSP char* bp = sm + (c ? K2_Z - (r + 1) * K2_UBLK : K2_U0 + r * K2_UBLK) + 16 * h;
        f32x16 y = {};
        for (int m = 0; m < 32; ++m) {
#pragma unroll
            for (int k = 0; k < 4; ++k) {
                const u32x4a a = *(const LDSP u32x4a*)(ap + 96 - 32 * k);
                const bf16x8 bb = *(const LDSP bf16x8*)(bp + 32 * k);
                y = mfma_bf16(as_bf((u32x4)a), bb, y);
            }
            ap += 128; bp += ustep;
        }
        STAMP(5);
        const LDSP char* up = sm + (c ? K2_Z - (r + 1) * K2_UBLK : K2_U0 + r * K2_UBLK) + 64 * rho + 8 * (1 - h);
        unsigned short* op = out + (size_t)(64 * r + 32 * rho + 4 * h) * 1024 + ch;
#pragma unroll
        for (int gq = 0; gq < 4; ++gq) {
            const u32x2 uu = *(const LDSP u32x2*)(up + 16 * gq);
#pragma unroll
            for (int e = 0; e < 4; ++e) {
                const unsigned pr = uu[(3 - e) >> 1];
                const float uf = __uint_as_float(((3 - e) & 1) ? (pr & 0xffff0000u) : (pr << 16));
                const float res = bf16r(y[4 * gq + e]) + bf16r(uf * dsk);
                op[(size_t)(8 * gq + e) * 1024] = (unsigned short)(cvtpk_bf16(res, res) & 0xffffu);
            }
        }
        STAMP(6);
    }
    STAMP_OUT(1, ws);
}

constexpr unsigned K3_O = 0, K3_W = 65536, K3_LDS = 131072;

__global__ __launch_bounds__(256) void k3_oproj(const char* __restrict__ wso, const unsigned short* __restrict__ wo, unsigned short* __restrict__ out) {
    __shared__ __attribute__((aligned(16))) char smem[K3_LDS];
    LDSP char* const sm = (LDSP char*)smem;
    const unsigned lds0 = (unsigned)(uintptr_t)sm;
    const int lane = threadIdx.x & 63, w = __builtin_amdgcn_readfirstlane(threadIdx.x >> 6), b = blockIdx.x;
    const int r = lane & 31, h = lane >> 5;
    STAMP_DECL; STAMP(0);
    const int qp = (b & 7) * 4 + ((b >> 3) & 3), np = b >> 5;
    const char* og = wso + (size_t)qp * 65536;
    const char* wg = (const char*)wo + np * 128;
    const unsigned woff = (lane >> 2) * 1024 + (lane & 3) * 16;
#pragma unroll
    for (int ck = 0; ck < 4; ++ck) {
#pragma unroll
        for (int i = 0; i < 4; ++i) {
            const int blk = (i >> 1) * 32 + ck * 8 + (i & 1) * 4 + w;
            glds16(og + blk * 1024, lane * 16, lds0 + K3_O + blk * 1024);
            const int strip = i >> 1, kr = ck * 128 + ((i & 1) * 4 + w) * 16;
            glds16(wg + (size_t)kr * 1024 + strip * 64, woff, lds0 + K3_W + strip * 32768 + kr * 64);
        }
    }
    STAMP(1);
    const int wq = w & 1, wn = w >> 1;
    const LDSP char* bp = sm + K3_O + wq * 32768 + lane * 16;
    const LDSP char* ap = sm + K3_W + wn * 32768 + (4 * h + ((lane & 15) >> 2)) * 64 + (((lane >> 4) & 1) * 16 + (lane & 3) * 4) * 2;
    f32x16 acc = {};
#pragma unroll
    for (int ck = 0; ck < 4; ++ck) {
        if (ck == 0) WAIT_BAR(24); else if (ck == 1) WAIT_BAR(16); else if (ck == 2) WAIT_BAR(8); else WAIT_BAR(0);
        if (ck == 0) STAMP(2);
        if (ck == 3) STAMP(3);
#pragma unroll
        for (int i = 0; i < 8; ++i) {
            const int hd = ck * 8 + i;
            const bf16x8 a = tr_frag(ap, hd * 1024, hd * 1024 + 512);
            const bf16x8 bb = *(const LDSP bf16x8*)(bp + hd * 1024);
            acc = mfma_bf16(a, bb, acc);
        }
    }
    STAMP(4);
    char* op = (char*)out + (size_t)(qp * 64 + wq * 32 + r) * 2048 + 1024 + (np * 64 + wn * 32) * 2 + h * 16;
#pragma unroll
    for (int gp = 0; gp < 2; ++gp) {
        unsigned a0 = cvtpk_bf16(acc[8 * gp + 0], acc[8 * gp + 1]), a1 = cvtpk_bf16(acc[8 * gp + 2], acc[8 * gp + 3]);
        unsigned b0 = cvtpk_bf16(acc[8 * gp + 4], acc[8 * gp + 5]), b1 = cvtpk_bf16(acc[8 * gp + 6], acc[8 * gp + 7]);
        const auto s0 = __builtin_amdgcn_permlane32_swap(a0, b0, false, false);
        const auto s1 = __builtin_amdgcn_permlane32_swap(a1, b1, false, false);
        const u32x4 v = {s0[0], s1[0], s0[1], s1[1]};
        *(u32x4*)(op + gp * 32) = v;
    }
    STAMP(5);
    STAMP_OUT(2, (char*)wso - WS_O);
}

extern "C" void kernel_launch(void* const* d_in, const int* in_sizes, int n_in, void* d_out, int out_size, void* d_ws, size_t ws_size,
                              hipStream_t stream) {
    const unsigned short* u = (const unsigned short*)d_in[0];
    const unsigned short* logA = (const unsigned short*)d_in[1];
    const unsigned short* aim = (const unsigned short*)d_in[2];
    const unsigned short* cre = (const unsigned short*)d_in[3];
    const unsigned short* cim = (const unsigned short*)d_in[4];
    const unsigned short* logdt = (const unsigned short*)d_in[5];
    const unsigned short* dskip = (const unsigned short*)d_in[6];
    const unsigned short* x = (const unsigned short*)d_in[7];
    const unsigned short* wqkv = (const unsigned short*)d_in[8];
    const unsigned short* wo = (const unsigned short*)d_in[9];
    char* ws = (char*)d_ws;
    unsigned short* out = (unsigned short*)d_out;
    k1_qkv_taps<<<256, 512, 0, stream>>>(x, wqkv, logA, aim, cre, cim, logdt, ws);
    k2_attn_conv<<<256, 768, 0, stream>>>(u, dskip, ws, ws + WS_O, out);
    k3_oproj<<<256, 256, 0, stream>>>(ws + WS_O, wo, out);
}
```

```cpp
#include <hip/hip_runtime.h>
#include <hip/hip_bf16.h>
#include <stdint.h>

typedef __bf16 bf16x8 __attribute__((ext_vector_type(8)));
typedef __bf16 bf16x2 __attribute__((ext_vector_type(2)));
typedef _Float16 f16x8 __attribute__((ext_vector_type(8)));
typedef _Float16 f16x2 __attribute__((ext_vector_type(2)));
typedef float f32x16 __attribute__((ext_vector_type(16)));
typedef float f32x2 __attribute__((ext_vector_type(2)));
typedef unsigned u32x4 __attribute__((ext_vector_type(4)));
typedef unsigned u32x4a __attribute__((ext_vector_type(4), aligned(4)));
typedef unsigned u32x2 __attribute__((ext_vector_type(2)));
typedef short s16x4 __attribute__((ext_vector_type(4)));
typedef __attribute__((address_space(1))) unsigned long long gu64;
#define LDSP __attribute__((address_space(3)))
#define DI __device__ __forceinline__
#define RLX_AGENT __ATOMIC_RELAXED, __HIP_MEMORY_SCOPE_AGENT

constexpr size_t WS_K = 0;
constexpr size_t WS_V = WS_K + (size_t)32 * 65536;
constexpr size_t WS_O = WS_V + (size_t)32 * 65536;
constexpr size_t WS_KM = WS_O + (size_t)64 * 32768;
constexpr size_t WS_F1 = WS_KM + 32 * 64 * 4;
constexpr size_t WS_F2 = WS_F1 + 256 * 128;
constexpr size_t WS_EP = WS_F2 + 256 * 128;
constexpr unsigned VT = 1056;
constexpr unsigned TAP_C1 = 4288, TAP_CH = 8512, TAP_USED = 17024, UBLK = 144;
constexpr unsigned L_K = 0, L_V = 65536, L_TAP = L_V + 64 * VT, L_U1 = L_TAP + TAP_USED, L_Z = L_U1 + 32 * UBLK,
                   L_U0 = L_Z + 31 * UBLK, L_END = L_U0 + 32 * UBLK;
constexpr unsigned L_FLAG = L_TAP + TAP_CH + 4224;
constexpr unsigned L1_W = 0, L1_WV = 32768, L1_X = 49152, XSLOTS = 9;
constexpr unsigned P_ROW = 272, P_BUF = 32 * P_ROW, L_SSM0 = L1_X + 8 * XSLOTS * 1024, L_SSM2 = L_FLAG + 64;
constexpr unsigned L_TAB = L_U1, TAB_SZ = 2896;
constexpr unsigned L_QX = L_TAP;
constexpr unsigned L3_W = 0, L3_O = 131072, L_PART = 131072, PART_SZ = 2560;
constexpr unsigned LDS_BYTES = 163840;
static_assert(L_END <= LDS_BYTES && L_SSM0 + 2 * P_BUF <= L_FLAG && L_SSM2 + 2 * P_BUF <= LDS_BYTES && L_TAB + 4 * TAB_SZ <= LDS_BYTES, "LDS map");
constexpr float QSCALE = 0.25f * 1.4426950408889634f;
constexpr float SAFE_BOUND = 40.0f;
constexpr unsigned SPIN_CAP = 1u << 21;

#define STAMP_DECL
#define STAMP(i)
#define STAMPC(i)
#define STAMP_OUT(base)

DI float bf2f(unsigned short v) { return __uint_as_float((unsigned)v << 16); }
DI unsigned cvtpk_bf16(float lo, float hi) { f32x2 v = {lo, hi}; return __builtin_bit_cast(unsigned, __builtin_convertvector(v, bf16x2)); }
DI unsigned cvtpk_f16(float lo, float hi) { f32x2 v = {lo, hi}; return __builtin_bit_cast(unsigned, __builtin_convertvector(v, f16x2)); }
DI float bf16r(float x) { return __uint_as_float(cvtpk_bf16(x, x) << 16); }
DI f32x16 mfma_bf16(bf16x8 a, bf16x8 b, f32x16 c) { return __builtin_amdgcn_mfma_f32_32x32x16_bf16(a, b, c, 0, 0, 0); }
DI f32x16 mfma_f16(f16x8 a, f16x8 b, f32x16 c) { return __builtin_amdgcn_mfma_f32_32x32x16_f16(a, b, c, 0, 0, 0); }
DI bf16x8 as_bf(u32x4 v) { return __builtin_bit_cast(bf16x8, v); }
DI void glds16(const void* sbase, unsigned voff, unsigned lds) {
    unsigned sv;
    asm volatile("s_mov_b32 %0, m0\n\ts_mov_b32 m0, %3\n\ts_nop 0\n\tglobal_load_lds_dwordx4 %1, %2\n\ts_mov_b32 m0, %0"
                 : "=&s"(sv) : "v"(voff), "s"(sbase), "s"(lds) : "memory");
}
DI void glds16_dev(const void* sbase, unsigned voff, unsigned lds) {
    unsigned sv;
    asm volatile("s_mov_b32 %0, m0\n\ts_mov_b32 m0, %3\n\ts_nop 0\n\tglobal_load_lds_dwordx4 %1, %2 sc1\n\ts_mov_b32 m0, %0"
                 : "=&s"(sv) : "v"(voff), "s"(sbase), "s"(lds) : "memory");
}
DI void store16_dev(void* sbase, unsigned voff, u32x4 v) {
    asm volatile("global_store_dwordx4 %0, %1, %2 sc1\n\ts_nop 1" :: "v"(voff), "v"(v), "s"(sbase) : "memory");
}
DI void store4_dev(void* sbase, unsigned voff, float v) {
    asm volatile("global_store_dword %0, %1, %2 sc1" :: "v"(voff), "v"(v), "s"(sbase) : "memory");
}
#define WAIT_VM(n) asm volatile("s_waitcnt vmcnt(" #n ")" ::: "memory")
#define WAIT_LGKM() asm volatile("s_waitcnt lgkmcnt(0)" ::: "memory")
#define WAIT_BAR(n) asm volatile("s_waitcnt vmcnt(" #n ") lgkmcnt(0)\n\ts_barrier" ::: "memory")
#define BAR_LGKM() asm volatile("s_waitcnt lgkmcnt(0)\n\ts_barrier" ::: "memory")
template <int CTRL> DI float dpp_max(float v) {
    const int o = __builtin_amdgcn_update_dpp(__float_as_int(v), __float_as_int(v), CTRL, 0xf, 0xf, false);
    return fmaxf(v, __int_as_float(o));
}
DI float wave_max(float v) {
    v = dpp_max<0x111>(v); v = dpp_max<0x112>(v); v = dpp_max<0x114>(v); v = dpp_max<0x118>(v);
    v = dpp_max<0x142>(v); v = dpp_max<0x143>(v);
    return __int_as_float(__builtin_amdgcn_readlane(__float_as_int(v), 63));
}
DI float half_sum(float v) {
    auto r = __builtin_amdgcn_permlane32_swap(__float_as_uint(v), __float_as_uint(v), false, false);
    return __uint_as_float(r[0]) + __uint_as_float(r[1]);
}
DI float half_max(float v) {
    auto r = __builtin_amdgcn_permlane32_swap(__float_as_uint(v), __float_as_uint(v), false, false);
    return fmaxf(__uint_as_float(r[0]), __uint_as_float(r[1]));
}
DI u32x4 pack8(const f32x16& a, int o, float s) {
    u32x4 r;
    r[0] = cvtpk_bf16(a[o + 0] * s, a[o + 1] * s); r[1] = cvtpk_bf16(a[o + 2] * s, a[o + 3] * s);
    r[2] = cvtpk_bf16(a[o + 4] * s, a[o + 5] * s); r[3] = cvtpk_bf16(a[o + 6] * s, a[o + 7] * s);
    return r;
}
DI float norm2_8(u32x4 v) {
    float n = 0.f;
#pragma unroll
    for (int i = 0; i < 4; ++i) { const float lo = __uint_as_float(v[i] << 16), hi = __uint_as_float(v[i] & 0xffff0000u); n = fmaf(lo, lo, fmaf(hi, hi, n)); }
    return n;
}
DI bf16x8 tr_frag(const LDSP char* p, int off_lo, int off_hi) {
    const s16x4 lo = __builtin_amdgcn_ds_read_tr16_b64_v4i16((LDSP s16x4*)(p + off_lo));
    const s16x4 hi = __builtin_amdgcn_ds_read_tr16_b64_v4i16((LDSP s16x4*)(p + off_hi));
    typedef short s16x8 __attribute__((ext_vector_type(8)));
    const s16x8 v = __builtin_shufflevector(lo, hi, 0, 1, 2, 3, 4, 5, 6, 7);
    return __builtin_bit_cast(bf16x8, v);
}
DI void wait_tags(const gu64* p, unsigned long long tag) {
    for (unsigned spins = 0; spins < SPIN_CAP; ++spins) {
        const unsigned long long x = __hip_atomic_load(p, RLX_AGENT);
        if (__all(x == tag)) break;
        __builtin_amdgcn_s_sleep(1);
    }
    asm volatile("" ::: "memory");
}
DI unsigned wait_lds_word(const LDSP volatile unsigned* p, unsigned atleast) {
    unsigned v = 0;
    for (unsigned spins = 0; spins < SPIN_CAP; ++spins) {
        v = __builtin_amdgcn_readfirstlane(*p);
        if (v >= atleast) break;
        __builtin_amdgcn_s_sleep(2);
    }
    asm volatile("" ::: "memory");
    return v;
}
struct cplx { float r, i; };
DI float fmul1(float a, float b) { float r; asm("v_mul_f32 %0, %1, %2" : "=v"(r) : "v"(a), "v"(b)); return r; }
DI float ffma1(float a, float b, float c) { float r; asm("v_fma_f32 %0, %1, %2, %3" : "=v"(r) : "v"(a), "v"(b), "v"(c)); return r; }
DI float fnma1(float a, float b, float c) { float r; asm("v_fma_f32 %0, -%1, %2, %3" : "=v"(r) : "v"(a), "v"(b), "v"(c)); return r; }
DI cplx cmul(cplx a, cplx b) { cplx o; o.r = fnma1(a.i, b.i, fmul1(a.r, b.r)); o.i = ffma1(a.i, b.r, fmul1(a.r, b.i)); return o; }
DI cplx zpow(float xl, float yr, float p) {
    const float e = __builtin_amdgcn_exp2f(xl * p);
    const float th = __builtin_amdgcn_fractf(yr * p);
    cplx o; o.r = e * __builtin_amdgcn_cosf(th); o.i = e * __builtin_amdgcn_sinf(th); return o;
}
DI int tab_idx(int ci) { return ci + (ci >> 3); }

#define ATTN_ASM \
    "v_mov_b32 v48, 0\n\t" \
    "v_mov_b32 v49, 0\n\t" \
    "v_mov_b32 v50, 0\n\t" \
    "v_mov_b32 v51, 0\n\t" \
    "v_mov_b32 v56, 0\n\t" \
    "v_mov_b32 v57, 0\n\t" \
    "v_mov_b32 v58, 0\n\t" \
    "v_mov_b32 v59, 0\n\t" \
    "v_mov_b32 v92, 0\n\t" \
    "v_mov_b32 v93, 0\n\t" \
    "v_mov_b32 v94, 0\n\t" \
    "v_mov_b32 v95, 0\n\t" \
    "v_mov_b32 v96, 0\n\t" \
    "v_mov_b32 v97, 0\n\t" \
    "v_mov_b32 v98, 0\n\t" \
    "v_mov_b32 v99, 0\n\t" \
    "v_mov_b32 v100, 0\n\t" \
    "v_mov_b32 v101, 0\n\t" \
    "v_mov_b32 v102, 0\n\t" \
    "v_mov_b32 v103, 0\n\t" \
    "v_mov_b32 v104, 0\n\t" \
    "v_mov_b32 v105, 0\n\t" \
    "v_mov_b32 v106, 0\n\t" \
    "v_mov_b32 v107, 0\n\t" \
    "v_mov_b32 v108, 0\n\t" \
    "v_mov_b32 v109, 0\n\t" \
    "v_mov_b32 v110, 0\n\t" \
    "v_mov_b32 v111, 0\n\t" \
    "v_mov_b32 v112, 0\n\t" \
    "v_mov_b32 v113, 0\n\t" \
    "v_mov_b32 v114, 0\n\t" \
    "v_mov_b32 v115, 0\n\t" \
    "v_mov_b32 v116, 0\n\t" \
    "v_mov_b32 v117, 0\n\t" \
    "v_mov_b32 v118, 0\n\t" \
    "v_mov_b32 v119, 0\n\t" \
    "v_mov_b32 v120, 0\n\t" \
    "v_mov_b32 v121, 0\n\t" \
    "v_mov_b32 v122, 0\n\t" \
    "v_mov_b32 v123, 0\n\t" \
    "ds_read_b128 v[40:43], v124 offset:0\n\t" \
    "ds_read_b128 v[44:47], v125 offset:0\n\t" \
    "s_waitcnt lgkmcnt(1)\n\t" \
    "v_mfma_f32_32x32x16_bf16 v[60:75], v[40:43], v[32:35], 0\n\t" \
    "s_mov_b32 %[j], 0\n\t" \
    "s_nop 7\n\t" \
    "s_nop 3\n\t" \
    "LOOP_%=:\n\t" \
    "v_exp_f32 v60, v60\n\t" \
    "v_exp_f32 v61, v61\n\t" \
    "v_mfma_f32_32x32x16_bf16 v[108:123], v[48:51], v[56:59], v[108:123]\n\t" \
    "ds_read_b128 v[48:51], v126 offset:0\n\t" \
    "v_exp_f32 v62, v62\n\t" \
    "v_exp_f32 v63, v63\n\t" \
    "v_exp_f32 v64, v64\n\t" \
    "v_exp_f32 v65, v65\n\t" \
    "v_exp_f32 v66, v66\n\t" \
    "v_mfma_f32_32x32x16_bf16 v[76:91], v[40:43], v[36:39], 0\n\t" \
    "ds_read_b128 v[40:43], v124 offset:1024\n\t" \
    "v_exp_f32 v67, v67\n\t" \
    "v_cvt_pk_bf16_f32 v52, v60, v61\n\t" \
    "v_cvt_pk_bf16_f32 v53, v62, v63\n\t" \
    "v_cvt_pk_bf16_f32 v54, v64, v65\n\t" \
    "v_cvt_pk_bf16_f32 v55, v66, v67\n\t" \
    "v_exp_f32 v68, v68\n\t" \
    "v_exp_f32 v69, v69\n\t" \
    "v_exp_f32 v70, v70\n\t" \
    "v_exp_f32 v71, v71\n\t" \
    "s_waitcnt lgkmcnt(2)\n\t" \
    "v_mfma_f32_32x32x16_bf16 v[92:107], v[44:47], v[52:55], v[92:107]\n\t" \
    "v_exp_f32 v72, v72\n\t" \
    "v_exp_f32 v73, v73\n\t" \
    "v_exp_f32 v74, v74\n\t" \
    "v_exp_f32 v75, v75\n\t" \
    "v_cvt_pk_bf16_f32 v56, v68, v69\n\t" \
    "v_cvt_pk_bf16_f32 v57, v70, v71\n\t" \
    "v_cvt_pk_bf16_f32 v58, v72, v73\n\t" \
    "v_cvt_pk_bf16_f32 v59, v74, v75\n\t" \
    "v_exp_f32 v76, v76\n\t" \
    "v_exp_f32 v77, v77\n\t" \
    "s_waitcnt lgkmcnt(0)\n\t" \
    "v_mfma_f32_32x32x16_bf16 v[92:107], v[48:51], v[56:59], v[92:107]\n\t" \
    "v_exp_f32 v78, v78\n\t" \
    "v_exp_f32 v79, v79\n\t" \
    "v_exp_f32 v80, v80\n\t" \
    "v_exp_f32 v81, v81\n\t" \
    "v_exp_f32 v82, v82\n\t" \
    "v_mfma_f32_32x32x16_bf16 v[60:75], v[40:43], v[32:35], 0\n\t" \
    "v_exp_f32 v83, v83\n\t" \
    "v_cvt_pk_bf16_f32 v52, v76, v77\n\t" \
    "v_cvt_pk_bf16_f32 v53, v78, v79\n\t" \
    "v_cvt_pk_bf16_f32 v54, v80, v81\n\t" \
    "v_cvt_pk_bf16_f32 v55, v82, v83\n\t" \
    "v_exp_f32 v84, v84\n\t" \
    "v_exp_f32 v85, v85\n\t" \
    "v_exp_f32 v86, v86\n\t" \
    "v_exp_f32 v87, v87\n\t" \
    "v_mfma_f32_32x32x16_bf16 v[108:123], v[44:47], v[52:55], v[108:123]\n\t" \
    "ds_read_b128 v[44:47], v125 offset:1056\n\t" \
    "v_exp_f32 v88, v88\n\t" \
    "v_exp_f32 v89, v89\n\t" \
    "v_exp_f32 v90, v90\n\t" \
    "v_exp_f32 v91, v91\n\t" \
    "v_cvt_pk_bf16_f32 v56, v84, v85\n\t" \
    "v_cvt_pk_bf16_f32 v57, v86, v87\n\t" \
    "v_cvt_pk_bf16_f32 v58, v88, v89\n\t" \
    "v_cvt_pk_bf16_f32 v59, v90, v91\n\t" \
    "v_exp_f32 v60, v60\n\t" \
    "v_exp_f32 v61, v61\n\t" \
    "v_mfma_f32_32x32x16_bf16 v[108:123], v[48:51], v[56:59], v[108:123]\n\t" \
    "ds_read_b128 v[48:51], v126 offset:1056\n\t" \
    "v_exp_f32 v62, v62\n\t" \
    "v_exp_f32 v63, v63\n\t" \
    "v_exp_f32 v64, v64\n\t" \
    "v_exp_f32 v65, v65\n\t" \
    "v_exp_f32 v66, v66\n\t" \
    "v_mfma_f32_32x32x16_bf16 v[76:91], v[40:43], v[36:39], 0\n\t" \
    "ds_read_b128 v[40:43], v124 offset:2048\n\t" \
    "v_exp_f32 v67, v67\n\t" \
    "v_cvt_pk_bf16_f32 v52, v60, v61\n\t" \
    "v_cvt_pk_bf16_f32 v53, v62, v63\n\t" \
    "v_cvt_pk_bf16_f32 v54, v64, v65\n\t" \
    "v_cvt_pk_bf16_f32 v55, v66, v67\n\t" \
    "v_exp_f32 v68, v68\n\t" \
    "v_exp_f32 v69, v69\n\t" \
    "v_exp_f32 v70, v70\n\t" \
    "v_exp_f32 v71, v71\n\t" \
    "s_waitcnt lgkmcnt(2)\n\t" \
    "v_mfma_f32_32x32x16_bf16 v[92:107], v[44:47], v[52:55], v[92:107]\n\t" \
    "v_exp_f32 v72, v72\n\t" \
    "v_exp_f32 v73, v73\n\t" \
    "v_exp_f32 v74, v74\n\t" \
    "v_exp_f32 v75, v75\n\t" \
    "v_cvt_pk_bf16_f32 v56, v68, v69\n\t" \
    "v_cvt_pk_bf16_f32 v57, v70, v71\n\t" \
    "v_cvt_pk_bf16_f32 v58, v72, v73\n\t" \
    "v_cvt_pk_bf16_f32 v59, v74, v75\n\t" \
    "v_exp_f32 v76, v76\n\t" \
    "v_exp_f32 v77, v77\n\t" \
    "s_waitcnt lgkmcnt(0)\n\t" \
    "v_mfma_f32_32x32x16_bf16 v[92:107], v[48:51], v[56:59], v[92:107]\n\t" \
    "v_exp_f32 v78, v78\n\t" \
    "v_exp_f32 v79, v79\n\t" \
    "v_exp_f32 v80, v80\n\t" \
    "v_exp_f32 v81, v81\n\t" \
    "v_exp_f32 v82, v82\n\t" \
    "v_mfma_f32_32x32x16_bf16 v[60:75], v[40:43], v[32:35], 0\n\t" \
    "v_exp_f32 v83, v83\n\t" \
    "v_cvt_pk_bf16_f32 v52, v76, v77\n\t" \
    "v_cvt_pk_bf16_f32 v53, v78, v79\n\t" \
    "v_cvt_pk_bf16_f32 v54, v80, v81\n\t" \
    "v_cvt_pk_bf16_f32 v55, v82, v83\n\t" \
    "v_exp_f32 v84, v84\n\t" \
    "v_exp_f32 v85, v85\n\t" \
    "v_exp_f32 v86, v86\n\t" \
    "v_exp_f32 v87, v87\n\t" \
    "v_mfma_f32_32x32x16_bf16 v[108:123], v[44:47], v[52:55], v[108:123]\n\t" \
    "ds_read_b128 v[44:47], v125 offset:2112\n\t" \
    "v_exp_f32 v88, v88\n\t" \
    "v_exp_f32 v89, v89\n\t" \
    "v_exp_f32 v90, v90\n\t" \
    "v_exp_f32 v91, v91\n\t" \
    "v_cvt_pk_bf16_f32 v56, v84, v85\n\t" \
    "v_cvt_pk_bf16_f32 v57, v86, v87\n\t" \
    "v_cvt_pk_bf16_f32 v58, v88, v89\n\t" \
    "v_cvt_pk_bf16_f32 v59, v90, v91\n\t" \
    "v_exp_f32 v60, v60\n\t" \
    "v_exp_f32 v61, v61\n\t" \
    "v_mfma_f32_32x32x16_bf16 v[108:123], v[48:51], v[56:59], v[108:123]\n\t" \
    "ds_read_b128 v[48:51], v126 offset:2112\n\t" \
    "v_exp_f32 v62, v62\n\t" \
    "v_exp_f32 v63, v63\n\t" \
    "v_exp_f32 v64, v64\n\t" \
    "v_exp_f32 v65, v65\n\t" \
    "v_exp_f32 v66, v66\n\t" \
    "v_mfma_f32_32x32x16_bf16 v[76:91], v[40:43], v[36:39], 0\n\t" \
    "ds_read_b128 v[40:43], v124 offset:3072\n\t" \
    "v_exp_f32 v67, v67\n\t" \
    "v_cvt_pk_bf16_f32 v52, v60, v61\n\t" \
    "v_cvt_pk_bf16_f32 v53, v62, v63\n\t" \
    "v_cvt_pk_bf16_f32 v54, v64, v65\n\t" \
    "v_cvt_pk_bf16_f32 v55, v66, v67\n\t" \
    "v_exp_f32 v68, v68\n\t" \
    "v_exp_f32 v69, v69\n\t" \
    "v_exp_f32 v70, v70\n\t" \
    "v_exp_f32 v71, v71\n\t" \
    "s_waitcnt lgkmcnt(2)\n\t" \
    "v_mfma_f32_32x32x16_bf16 v[92:107], v[44:47], v[52:55], v[92:107]\n\t" \
    "v_exp_f32 v72, v72\n\t" \
    "v_exp_f32 v73, v73\n\t" \
    "v_exp_f32 v74, v74\n\t" \
    "v_exp_f32 v75, v75\n\t" \
    "v_cvt_pk_bf16_f32 v56, v68, v69\n\t" \
    "v_cvt_pk_bf16_f32 v57, v70, v71\n\t" \
    "v_cvt_pk_bf16_f32 v58, v72, v73\n\t" \
    "v_cvt_pk_bf16_f32 v59, v74, v75\n\t" \
    "v_exp_f32 v76, v76\n\t" \
    "v_exp_f32 v77, v77\n\t" \
    "s_waitcnt lgkmcnt(0)\n\t" \
    "v_mfma_f32_32x32x16_bf16 v[92:107], v[48:51], v[56:59], v[92:107]\n\t" \
    "v_exp_f32 v78, v78\n\t" \
    "v_exp_f32 v79, v79\n\t" \
    "v_exp_f32 v80, v80\n\t" \
    "v_exp_f32 v81, v81\n\t" \
    "v_exp_f32 v82, v82\n\t" \
    "v_mfma_f32_32x32x16_bf16 v[60:75], v[40:43], v[32:35], 0\n\t" \
    "v_exp_f32 v83, v83\n\t" \
    "v_cvt_pk_bf16_f32 v52, v76, v77\n\t" \
    "v_cvt_pk_bf16_f32 v53, v78, v79\n\t" \
    "v_cvt_pk_bf16_f32 v54, v80, v81\n\t" \
    "v_cvt_pk_bf16_f32 v55, v82, v83\n\t" \
    "v_exp_f32 v84, v84\n\t" \
    "v_exp_f32 v85, v85\n\t" \
    "v_exp_f32 v86, v86\n\t" \
    "v_exp_f32 v87, v87\n\t" \
    "v_mfma_f32_32x32x16_bf16 v[108:123], v[44:47], v[52:55], v[108:123]\n\t" \
    "ds_read_b128 v[44:47], v125 offset:3168\n\t" \
    "v_exp_f32 v88, v88\n\t" \
    "v_exp_f32 v89, v89\n\t" \
    "v_exp_f32 v90, v90\n\t" \
    "v_exp_f32 v91, v91\n\t" \
    "v_cvt_pk_bf16_f32 v56, v84, v85\n\t" \
    "v_cvt_pk_bf16_f32 v57, v86, v87\n\t" \
    "v_cvt_pk_bf16_f32 v58, v88, v89\n\t" \
    "v_cvt_pk_bf16_f32 v59, v90, v91\n\t" \
    "s_add_u32 %[pc], %[pc], 1\n\t" \
    "s_and_b32 %[pc], %[pc], 7\n\t" \
    "s_cmp_eq_u32 %[pc], 0\n\t" \
    "s_cselect_b32 %[dk], 0x10000, 0\n\t" \
    "s_cselect_b32 %[dv], 0x10800, 0\n\t" \
    "s_sub_u32 %[dk], 0x2000, %[dk]\n\t" \
    "s_sub_u32 %[dv], 0x2100, %[dv]\n\t" \
    "s_cmp_eq_u32 %[j], 7\n\t" \
    "s_cbranch_scc0 CHK_%=\n\t" \
    "s_mov_b32 %[dk], 0\n\t" \
    "s_mov_b32 %[dv], 0\n\t" \
    "s_branch GO_%=\n\t" \
    "CHK_%=:\n\t" \
    "v_add_u32 v127, 4, v127\n\t" \
    "s_mov_b32 %[spin], 0\n\t" \
    "SPIN_%=:\n\t" \
    "ds_read_b32 v128, v127\n\t" \
    "s_waitcnt lgkmcnt(0)\n\t" \
    "s_nop 0\n\t" \
    "v_readfirstlane_b32 %[tmp], v128\n\t" \
    "s_nop 1\n\t" \
    "s_cmp_lg_u32 %[tmp], 0\n\t" \
    "s_cbranch_scc1 RDY_%=\n\t" \
    "s_sleep 2\n\t" \
    "s_add_u32 %[spin], %[spin], 1\n\t" \
    "s_cmp_lt_u32 %[spin], 0x200000\n\t" \
    "s_cbranch_scc1 SPIN_%=\n\t" \
    "RDY_%=:\n\t" \
    "s_cmp_gt_u32 %[tmp], %[thr]\n\t" \
    "s_cbranch_scc1 BAIL_%=\n\t" \
    "GO_%=:\n\t" \
    "v_exp_f32 v60, v60\n\t" \
    "v_exp_f32 v61, v61\n\t" \
    "v_mfma_f32_32x32x16_bf16 v[108:123], v[48:51], v[56:59], v[108:123]\n\t" \
    "ds_read_b128 v[48:51], v126 offset:3168\n\t" \
    "v_exp_f32 v62, v62\n\t" \
    "v_exp_f32 v63, v63\n\t" \
    "v_exp_f32 v64, v64\n\t" \
    "v_exp_f32 v65, v65\n\t" \
    "v_exp_f32 v66, v66\n\t" \
    "v_mfma_f32_32x32x16_bf16 v[76:91], v[40:43], v[36:39], 0\n\t" \
    "v_add_u32 v124, %[dk], v124\n\t" \
    "ds_read_b128 v[40:43], v124 offset:0\n\t" \
    "v_exp_f32 v67, v67\n\t" \
    "v_cvt_pk_bf16_f32 v52, v60, v61\n\t" \
    "v_cvt_pk_bf16_f32 v53, v62, v63\n\t" \
    "v_cvt_pk_bf16_f32 v54, v64, v65\n\t" \
    "v_cvt_pk_bf16_f32 v55, v66, v67\n\t" \
    "v_exp_f32 v68, v68\n\t" \
    "v_exp_f32 v69, v69\n\t" \
    "v_exp_f32 v70, v70\n\t" \
    "v_exp_f32 v71, v71\n\t" \
    "s_waitcnt lgkmcnt(2)\n\t" \
    "v_mfma_f32_32x32x16_bf16 v[92:107], v[44:47], v[52:55], v[92:107]\n\t" \
    "v_exp_f32 v72, v72\n\t" \
    "v_exp_f32 v73, v73\n\t" \
    "v_exp_f32 v74, v74\n\t" \
    "v_exp_f32 v75, v75\n\t" \
    "v_cvt_pk_bf16_f32 v56, v68, v69\n\t" \
    "v_cvt_pk_bf16_f32 v57, v70, v71\n\t" \
    "v_cvt_pk_bf16_f32 v58, v72, v73\n\t" \
    "v_cvt_pk_bf16_f32 v59, v74, v75\n\t" \
    "v_exp_f32 v76, v76\n\t" \
    "v_exp_f32 v77, v77\n\t" \
    "s_waitcnt lgkmcnt(0)\n\t" \
    "v_mfma_f32_32x32x16_bf16 v[92:107], v[48:51], v[56:59], v[92:107]\n\t" \
    "v_exp_f32 v78, v78\n\t" \
    "v_exp_f32 v79, v79\n\t" \
    "v_exp_f32 v80, v80\n\t" \
    "v_exp_f32 v81, v81\n\t" \
    "v_exp_f32 v82, v82\n\t" \
    "v_mfma_f32_32x32x16_bf16 v[60:75], v[40:43], v[32:35], 0\n\t" \
    "v_exp_f32 v83, v83\n\t" \
    "v_cvt_pk_bf16_f32 v52, v76, v77\n\t" \
    "v_cvt_pk_bf16_f32 v53, v78, v79\n\t" \
    "v_cvt_pk_bf16_f32 v54, v80, v81\n\t" \
    "v_cvt_pk_bf16_f32 v55, v82, v83\n\t" \
    "v_exp_f32 v84, v84\n\t" \
    "v_exp_f32 v85, v85\n\t" \
    "v_exp_f32 v86, v86\n\t" \
    "v_exp_f32 v87, v87\n\t" \
    "v_mfma_f32_32x32x16_bf16 v[108:123], v[44:47], v[52:55], v[108:123]\n\t" \
    "v_add_u32 v125, %[dv], v125\n\t" \
    "ds_read_b128 v[44:47], v125 offset:0\n\t" \
    "v_add_u32 v126, %[dv], v126\n\t" \
    "v_exp_f32 v88, v88\n\t" \
    "v_exp_f32 v89, v89\n\t" \
    "v_exp_f32 v90, v90\n\t" \
    "v_exp_f32 v91, v91\n\t" \
    "v_cvt_pk_bf16_f32 v56, v84, v85\n\t" \
    "v_cvt_pk_bf16_f32 v57, v86, v87\n\t" \
    "v_cvt_pk_bf16_f32 v58, v88, v89\n\t" \
    "v_cvt_pk_bf16_f32 v59, v90, v91\n\t" \
    "s_add_u32 %[j], %[j], 1\n\t" \
    "s_cmp_lt_u32 %[j], 8\n\t" \
    "s_cbranch_scc1 LOOP_%=\n\t" \
    "s_waitcnt lgkmcnt(0)\n\t" \
    "v_mfma_f32_32x32x16_bf16 v[108:123], v[48:51], v[56:59], v[108:123]\n\t" \
    "s_mov_b32 %[st], 0\n\t" \
    "s_branch END_%=\n\t" \
    "BAIL_%=:\n\t" \
    "s_waitcnt lgkmcnt(0)\n\t" \
    "s_mov_b32 %[st], 1\n\t" \
    "END_%=:\n\t" \
    "s_nop 15\n\t" \
    "s_nop 3\n\t"

__global__ __launch_bounds__(768) void kf_fused(const unsigned short* __restrict__ u, const unsigned short* __restrict__ logA,
                                                const unsigned short* __restrict__ aim, const unsigned short* __restrict__ cre,
                                                const unsigned short* __restrict__ cim, const unsigned short* __restrict__ logdt,
                                                const unsigned short* __restrict__ dskip, const unsigned short* __restrict__ x,
                                                const unsigned short* __restrict__ wqkv, const unsigned short* __restrict__ wo,
                                                char* __restrict__ ws, unsigned short* __restrict__ out) {
    __shared__ __attribute__((aligned(16))) char smem[LDS_BYTES];
    LDSP char* const sm = (LDSP char*)smem;
    const unsigned lds0 = (unsigned)(uintptr_t)sm;
    const int lane = threadIdx.x & 63, w = __builtin_amdgcn_readfirstlane(threadIdx.x >> 6), b = blockIdx.x;
    const int r = lane & 31, h = lane >> 5;
#define MAP 2
    const int rb = 4 * (b & 1) + (b >> 6), head = 8 * ((b >> 1) & 3) + ((b >> 3) & 7);
    const int wg = head * 8 + rb;
    STAMP_DECL; STAMP(0);
    u32x2 epv;
    asm volatile("global_load_dwordx2 %0, %1, %2 sc1" : "=&v"(epv) : "v"(0), "s"(ws + WS_EP) : "memory");
    auto get_tag = [&]() {
        asm volatile("" : "+v"(epv));
        return (((unsigned long long)(unsigned)__builtin_amdgcn_readfirstlane(epv[1]) << 32) | (unsigned)__builtin_amdgcn_readfirstlane(epv[0])) + 1;
    };
    if (w < 8) {
#define X_DMA_GUARD
        const unsigned wqk_o = (lane >> 2) * 3072 + ((lane >> 1) & 1) * 1024 + (lane & 1) * 16, wv_o = (lane >> 1) * 3072 + 2048 + (lane & 1) * 16;
        const char* wb = (const char*)wqkv + head * 32;
        const int rot = head & 7;
        auto issue_qk = [&](int p) { const int pp = (p + 4 * rot) & 31; X_DMA_GUARD glds16(wb + (size_t)pp * 16 * 3072, wqk_o, lds0 + L1_W + pp * 1024); };
        auto issue_v = [&](int p) { const int pp = (p + 2 * rot) & 15; X_DMA_GUARD glds16(wb + (size_t)pp * 32 * 3072, wv_o, lds0 + L1_WV + pp * 1024); };
        unsigned xoff[4];
#pragma unroll
        for (int i = 0; i < 4; ++i) { const int rr = 8 * i + (lane >> 3); xoff[i] = rr * 1024 + (((lane & 7) ^ ((rr >> 1) & 7)) << 4); }
        const char* xb = (const char*)x + (size_t)(rb * 256 + w * 32) * 1024;
        const unsigned xring = lds0 + L1_X + w * (XSLOTS * 1024);
        auto issue = [&](int g) { X_DMA_GUARD glds16(xb + (((g >> 2) + rot) & 7) * 128, xoff[g & 3], xring + (g % XSLOTS) * 1024); };
        asm volatile("s_barrier" ::: "memory");
        STAMP(14);
        issue_qk(w); if (w < 4) issue_v(w);
#pragma unroll
        for (int g = 0; g < XSLOTS; ++g) issue(g);
#pragma unroll
        for (int i = 0; i < 3; ++i) issue_qk(8 + 3 * w + i);
        issue_v(4 + w); if (w < 4) issue_v(12 + w);
        STAMP(1);
        const unsigned xrow = L1_X + w * (XSLOTS * 1024) + (r & 7) * 128;
        const unsigned wa = L1_W + (8 * h + ((lane & 15) >> 2)) * 64 + (((lane >> 4) & 1) * 16 + (lane & 3) * 4) * 2;
        const unsigned wva = L1_WV + (8 * h + ((lane & 15) >> 2)) * 32 + (lane & 3) * 8;
        f32x16 aqk = {}, av = {};
        bf16x8 wq[2][4], wv[2][4];
        auto read_w = [&](int s, int t) {
            const int sr = (s + rot) & 7;
            wq[s & 1][t] = tr_frag(sm + wa + sr * 4096, t * 1024, t * 1024 + 256);
            wv[s & 1][t] = tr_frag(sm + wva + sr * 2048, t * 512, t * 512 + 128);
        };
#pragma unroll
        for (int s = 0; s < 8; ++s) {
            if (s == 0) WAIT_BAR(9);
            else if (s == 1) WAIT_BAR(4);
            else if (s <= 5) WAIT_VM(5); else if (s == 6) WAIT_VM(4); else WAIT_VM(0);
            if (s == 0) STAMP(2);
            if (s == 4) STAMP(3);
            const unsigned slot = (4 * s) % XSLOTS + (r >> 3);
            const LDSP char* xp = sm + xrow + (slot >= XSLOTS ? slot - XSLOTS : slot) * 1024;
            bf16x8 xf[4];
#pragma unroll
            for (int t = 0; t < 4; ++t) xf[t] = *(const LDSP bf16x8*)(xp + (((2 * t + h) ^ ((r >> 1) & 7)) << 4));
            if (s == 0) {
#pragma unroll
                for (int t = 0; t < 4; ++t) read_w(0, t);
            }
            asm volatile("s_waitcnt lgkmcnt(0)" : "+v"(xf[0]), "+v"(xf[1]), "+v"(xf[2]), "+v"(xf[3]) :: "memory");
#pragma unroll
            for (int t = 0; t < 4; ++t) {
                __builtin_amdgcn_sched_barrier(0);
                aqk = mfma_bf16(wq[s & 1][t], xf[t], aqk);
                av = mfma_bf16(xf[t], wv[s & 1][t], av);
                __builtin_amdgcn_sched_barrier(0);
                if (s < 7) read_w(s + 1, t);
                if (XSLOTS + 4 * s + t < 32) issue(XSLOTS + 4 * s + t);
            }
        }
        STAMP(4);
        const unsigned long long tag = get_tag();
        BAR_LGKM();
        const int T = rb * 8 + w;
        const u32x4 qown = pack8(aqk, 0, QSCALE), kfr = pack8(aqk, 8, 1.0f);
        *(LDSP u32x4*)(sm + L_K + T * 1024 + lane * 16) = kfr;
        *(LDSP u32x4*)(sm + L_QX + w * 1024 + lane * 16) = qown;
        float vmx = 0.f;
        if (r < 16) {
            const u32x4 v0 = pack8(av, 0, 1.0f), v1 = pack8(av, 8, 1.0f);
            *(LDSP u32x4*)(sm + L_V + T * VT + h * 256 + r * 16) = v0;
            *(LDSP u32x4*)(sm + L_V + T * VT + 512 + h * 256 + r * 16) = v1;
#pragma unroll
            for (int i = 0; i < 16; ++i) vmx = fmaxf(vmx, fabsf(av[i]));
        }
        float km = wave_max(half_sum(norm2_8(kfr)));
        if (!(wave_max(vmx) <= 0x1p60f)) km = INFINITY;
        if (lane == 0) *(LDSP float*)(sm + L_QX + 8192 + w * 4) = km;
        BAR_LGKM();
        const u32x4 qoth = *(const LDSP u32x4*)(sm + L_QX + (w ^ 1) * 1024 + lane * 16);
        const float km_own = wave_max(*(const LDSP float*)(sm + L_QX + 8192 + (lane & 7) * 4));
        BAR_LGKM();
        STAMP(5); STAMPC(12);
        const int kh = w & 1;
        const u32x4 qa = kh ? qoth : qown, qb = kh ? qown : qoth;
        const float qn = wave_max(fmaxf(half_sum(norm2_8(qa)), half_sum(norm2_8(qb))));
        const float kthr_f = fminf(SAFE_BOUND * SAFE_BOUND / (1.0404f * qn), 3.0e38f);
        const unsigned thr = __builtin_amdgcn_readfirstlane(__float_as_uint(kthr_f));
        const unsigned t0 = 8 * rb + 4 * kh;
        const unsigned ka0 = lds0 + L_K + t0 * 1024 + lane * 16;
        const unsigned vpad = r == 16 ? 1024 : 1040;
        const unsigned va0 = lds0 + L_V + t0 * VT + (r < 16 ? h * 256 + r * 16 : vpad), va1 = lds0 + L_V + t0 * VT + (r < 16 ? 512 + h * 256 + r * 16 : vpad);
        f32x16 oa, ob;
        float ma = 0.f, mb = 0.f;
        unsigned st = !(km_own <= kthr_f);
        if (!st) {
            unsigned vk = ka0, vv0 = va0, vv1 = va1, vf = lds0 + L_FLAG;
            unsigned sj, spc = rb, sdk, sdv, sspin, stmp;
            u32x4 qa_ = qa, qb_ = qb;
            asm volatile(ATTN_ASM
                         : "+{v[32:35]}"(qa_), "+{v[36:39]}"(qb_), "={v[92:107]}"(oa), "={v[108:123]}"(ob),
                           "+{v124}"(vk), "+{v125}"(vv0), "+{v126}"(vv1), "+{v127}"(vf),
                           [j] "=&s"(sj), [pc] "+s"(spc), [dk] "=&s"(sdk), [dv] "=&s"(sdv), [spin] "=&s"(sspin), [tmp] "=&s"(stmp), [st] "=&s"(st)
                         : [thr] "s"(thr)
                         : "v40", "v41", "v42", "v43", "v44", "v45", "v46", "v47", "v48", "v49", "v50", "v51", "v52", "v53", "v54", "v55", "v56", "v57",
                           "v58", "v59", "v60", "v61", "v62", "v63", "v64", "v65", "v66", "v67", "v68", "v69", "v70", "v71", "v72", "v73", "v74", "v75",
                           "v76", "v77", "v78", "v79", "v80", "v81", "v82", "v83", "v84", "v85", "v86", "v87", "v88", "v89", "v90", "v91", "v128",
                           "scc", "memory");
        }
        if (st) {
#pragma unroll 1
            for (int j = 1; j < 8; ++j) wait_lds_word((const LDSP volatile unsigned*)(sm + L_FLAG + 4 * j), 1);
            const LDSP char* kp = sm + L_K + 4 * kh * 1024 + lane * 16;
            const LDSP char* vp0 = sm + L_V + 4 * kh * VT + (r < 16 ? h * 256 + r * 16 : vpad);
            const LDSP char* vp1 = sm + L_V + 4 * kh * VT + (r < 16 ? 512 + h * 256 + r * 16 : vpad);
            const f32x16 zero = {};
            ma = -INFINITY; mb = -INFINITY;
#pragma unroll 1
            for (int i = 0; i < 32; ++i) {
                const int t = (i >> 2) * 8 + (i & 3);
                const bf16x8 kf = *(const LDSP bf16x8*)(kp + t * 1024);
                const f32x16 sa = mfma_bf16(kf, as_bf(qa), zero), sb = mfma_bf16(kf, as_bf(qb), zero);
#pragma unroll
                for (int e = 0; e < 16; ++e) { ma = fmaxf(ma, sa[e]); mb = fmaxf(mb, sb[e]); }
            }
            ma = half_max(ma); mb = half_max(mb);
            oa = zero; ob = zero;
#pragma unroll 1
            for (int i = 0; i < 32; ++i) {
                const int t = (i >> 2) * 8 + (i & 3);
                const bf16x8 kf = *(const LDSP bf16x8*)(kp + t * 1024);
                const bf16x8 v0 = *(const LDSP bf16x8*)(vp0 + t * VT), v1 = *(const LDSP bf16x8*)(vp1 + t * VT);
                const f32x16 sa = mfma_bf16(kf, as_bf(qa), zero), sb = mfma_bf16(kf, as_bf(qb), zero);
                u32x4 p0, p1;
#pragma unroll
                for (int e = 0; e < 4; ++e) {
                    p0[e] = cvtpk_bf16(__builtin_amdgcn_exp2f(sa[2 * e] - ma), __builtin_amdgcn_exp2f(sa[2 * e + 1] - ma));
                    p1[e] = cvtpk_bf16(__builtin_amdgcn_exp2f(sa[8 + 2 * e] - ma), __builtin_amdgcn_exp2f(sa[9 + 2 * e] - ma));
                }
                oa = mfma_bf16(v0, as_bf(p0), oa); oa = mfma_bf16(v1, as_bf(p1), oa);
#pragma unroll
                for (int e = 0; e < 4; ++e) {
                    p0[e] = cvtpk_bf16(__builtin_amdgcn_exp2f(sb[2 * e] - mb), __builtin_amdgcn_exp2f(sb[2 * e + 1] - mb));
                    p1[e] = cvtpk_bf16(__builtin_amdgcn_exp2f(sb[8 + 2 * e] - mb), __builtin_amdgcn_exp2f(sb[9 + 2 * e] - mb));
                }
                ob = mfma_bf16(v0, as_bf(p0), ob); ob = mfma_bf16(v1, as_bf(p1), ob);
            }
        }
        STAMP(6); STAMPC(13);
        BAR_LGKM();
        STAMP(7);
        {
            const f32x16& og = kh ? oa : ob;
            const float mg = kh ? ma : mb;
            LDSP float* pp = (LDSP float*)(sm + L_PART + w * PART_SZ) + lane;
#pragma unroll
            for (int i = 0; i < 8; ++i) pp[i * 64] = og[i];
            pp[8 * 64] = og[8]; pp[9 * 64] = mg;
        }
        BAR_LGKM();
        {
            const f32x16& ok = kh ? ob : oa;
            const float mk = kh ? mb : ma;
            const LDSP float* pp = (const LDSP float*)(sm + L_PART + (w ^ 1) * PART_SZ) + lane;
            const float mo = pp[9 * 64], mm = fmaxf(mk, mo);
            const float fk = __builtin_amdgcn_exp2f(mk - mm), fo = __builtin_amdgcn_exp2f(mo - mm);
            const float lsum = half_sum(h == 0 ? ok[8] * fk + pp[8 * 64] * fo : 0.f);
            const float rl = 1.0f / lsum;
            f32x16 of;
#pragma unroll
            for (int i = 0; i < 8; ++i) of[i] = ok[i] * fk + pp[i * 64] * fo;
            store16_dev(ws + WS_O + ((size_t)T * 32 + head) * 1024, lane * 16, pack8(of, 0, rl));
        }
        WAIT_BAR(0);
        STAMP(8);
        if (w == 0) {
            if (lane == 0) __hip_atomic_store((gu64*)(ws + WS_F2 + wg * 128), tag, RLX_AGENT);
            wait_tags((const gu64*)(ws + WS_F2 + (rb + 8 * (lane & 31)) * 128), tag);
            if (b == 0 && lane == 0) __hip_atomic_store((gu64*)(ws + WS_EP), tag, RLX_AGENT);
        }
        asm volatile("s_barrier" ::: "memory");
        STAMP(9);
        const int qt3 = rb * 8 + 2 * (head >> 3) + ((head >> 2) & 1), fg = head & 3;
        {
            const char* og = ws + WS_O + ((size_t)qt3 * 32 + 4 * w) * 1024;
            u32x4 o0, o1, o2, o3;
            asm volatile("global_load_dwordx4 %0, %4, %5 sc1\n\tglobal_load_dwordx4 %1, %4, %5 offset:1024 sc1\n\t"
                         "global_load_dwordx4 %2, %4, %5 offset:2048 sc1\n\tglobal_load_dwordx4 %3, %4, %5 offset:3072 sc1\n\ts_waitcnt vmcnt(0)"
                         : "=&v"(o0), "=&v"(o1), "=&v"(o2), "=&v"(o3) : "v"(lane * 16), "s"(og) : "memory");
            LDSP char* od = sm + L3_O + 4 * w * 1024 + lane * 16;
            *(LDSP u32x4*)(od) = o0; *(LDSP u32x4*)(od + 1024) = o1; *(LDSP u32x4*)(od + 2048) = o2; *(LDSP u32x4*)(od + 3072) = o3;
        }
        BAR_LGKM();
        STAMP(10);
        if (w < 4) {
            const LDSP char* bp = sm + L3_O + lane * 16;
            const LDSP char* ap = sm + L3_W + w * 32768 + (4 * h + ((lane & 15) >> 2)) * 64 + (((lane >> 4) & 1) * 16 + (lane & 3) * 4) * 2;
            f32x16 acc = {};
#pragma unroll 8
            for (int hd = 0; hd < 32; ++hd) acc = mfma_bf16(tr_frag(ap, hd * 1024, hd * 1024 + 512), *(const LDSP bf16x8*)(bp + hd * 1024), acc);
            char* op = (char*)out + (size_t)(qt3 * 32 + r) * 2048 + 1024 + (fg * 128 + w * 32) * 2 + h * 16;
#pragma unroll
            for (int gp = 0; gp < 2; ++gp) {
                const unsigned a0 = cvtpk_bf16(acc[8 * gp + 0], acc[8 * gp + 1]), a1 = cvtpk_bf16(acc[8 * gp + 2], acc[8 * gp + 3]);
                const unsigned b0 = cvtpk_bf16(acc[8 * gp + 4], acc[8 * gp + 5]), b1 = cvtpk_bf16(acc[8 * gp + 6], acc[8 * gp + 7]);
                const auto s0 = __builtin_amdgcn_permlane32_swap(a0, b0, false, false);
                const auto s1 = __builtin_amdgcn_permlane32_swap(a1, b1, false, false);
                const u32x4 v = {s0[0], s1[0], s0[1], s1[1]};
                *(u32x4*)(op + gp * 32) = v;
            }
        }
        STAMP(11);
    } else {
        const int hw = w - 8, c = hw >> 1, cp = 32 * (b & 7) + (b >> 3), ch = 2 * cp + c, n = (hw & 1) * 64 + lane, ht = threadIdx.x - 512;
        LDSP char* buf = sm + (hw < 2 ? L_SSM0 + hw * P_BUF : L_SSM2 + (hw - 2) * P_BUF);
        LDSP unsigned* const cnt = (LDSP unsigned*)(sm + L_FLAG + 32);
        auto helper_sync = [&](int i) { WAIT_LGKM(); if (lane == 0) __hip_atomic_fetch_add(cnt + i, 1u, __ATOMIC_RELAXED, __HIP_MEMORY_SCOPE_WORKGROUP); wait_lds_word((const LDSP volatile unsigned*)(cnt + i), 4); };
        const float f_logA = bf2f(logA[ch * 128 + n]), f_aim = bf2f(aim[ch * 128 + n]);
        const float f_cre = bf2f(cre[ch * 128 + n]), f_cim = bf2f(cim[ch * 128 + n]), f_ldt = bf2f(logdt[ch]);
        const float dsk = bf2f(dskip[ch]);
        if (ht < 16) *(LDSP unsigned*)(sm + L_FLAG + ht * 4) = 0;
        asm volatile("s_barrier" ::: "memory");
#define KGEN_PRIO 2
        __builtin_amdgcn_s_setprio(KGEN_PRIO);
        const float dt = bf16r(expf(f_ldt));
        const float xl = -bf16r(expf(f_logA)) * dt * 1.4426950408889634f;
        const float yr = f_aim * dt * 0.15915494309189535f;
        cplx C; C.r = 2.0f * (f_cre * dt); C.i = 2.0f * (f_cim * dt);
        const int ex = __builtin_amdgcn_frexp_expf(wave_max(fmaxf(fabsf(C.r), fabsf(C.i))));
        C.r = ldexpf(C.r, -ex); C.i = ldexpf(C.i, -ex);
        const cplx z1 = zpow(xl, yr, 1.f), z8 = zpow(xl, yr, 8.f), z256 = zpow(xl, yr, 256.f), z2 = cmul(z1, z1), z4 = cmul(z2, z2);
        STAMP(1);
        BAR_LGKM();
        STAMP(2);
        cplx v; v.r = 1.f; v.i = 0.f;
        LDSP unsigned* row = (LDSP unsigned*)(buf + lane * 4);
#pragma unroll
        for (int i = 0; i < 32; ++i) {
            row[i * (P_ROW / 4)] = cvtpk_f16(v.r, v.i);
            v = cmul(v, z8);
            if (i == 15) BAR_LGKM();
        }
        STAMP(14);
        f16x8 bfr[8];
#pragma unroll
        for (int t = 0; t < 8; ++t) bfr[t] = *(const LDSP f16x8*)(buf + r * P_ROW + t * 32 + h * 16);
        {
            cplx a = C;
#pragma unroll
            for (int i = 0; i < 8; ++i) { row[i * (P_ROW / 4)] = cvtpk_f16(a.r, -a.i); a = cmul(a, z1); }
            cplx gk = cmul(C, z256); a = gk;
#pragma unroll
            for (int i = 0; i < 4; ++i) { row[(8 + i) * (P_ROW / 4)] = cvtpk_f16(a.r, -a.i); a = cmul(a, z2); }
            gk = cmul(gk, z256); row[12 * (P_ROW / 4)] = cvtpk_f16(gk.r, -gk.i); a = cmul(gk, z4); row[13 * (P_ROW / 4)] = cvtpk_f16(a.r, -a.i);
            gk = cmul(gk, z256); row[14 * (P_ROW / 4)] = cvtpk_f16(gk.r, -gk.i); a = cmul(gk, z4); row[15 * (P_ROW / 4)] = cvtpk_f16(a.r, -a.i);
#pragma unroll
            for (int i = 0; i < 5; ++i) { gk = cmul(gk, z256); row[(16 + i) * (P_ROW / 4)] = cvtpk_f16(gk.r, -gk.i); }
        }
        STAMP(15);
        f32x16 d = {};
#pragma unroll
        for (int t = 0; t < 8; ++t) d = mfma_f16(*(const LDSP f16x8*)(buf + r * P_ROW + t * 32 + h * 16), bfr[t], d);
        STAMP(3);
        helper_sync(2);
        {
            LDSP float* tc = (LDSP float*)(sm + L_TAB + hw * TAB_SZ);
#pragma unroll
            for (int e = 0; e < 4; ++e) {
                tc[tab_idx(8 * r + 4 * h + e)] = ldexpf(d[e], ex);
                tc[tab_idx(h ? 384 + 64 * (e >> 1) + 2 * r + (e & 1) : 256 + 4 * r + e)] = ldexpf(d[4 + e], ex);
                if (h == 0) tc[tab_idx(512 + 32 * e + r)] = ldexpf(d[8 + e], ex);
            }
            if (lane == 32) tc[tab_idx(640)] = ldexpf(d[8], ex);
        }
        STAMP(4);
        WAIT_VM(0);
        const unsigned long long tag = get_tag();
        BAR_LGKM();
        __builtin_amdgcn_s_setprio(3);
        if (hw == 0) {
            const u32x4 ones = {0x3F803F80u, 0x3F803F80u, 0x3F803F80u, 0x3F803F80u}, z = {0, 0, 0, 0};
            *(LDSP u32x4*)(sm + L_V + lane * VT + 1024) = ones;
            *(LDSP u32x4*)(sm + L_V + lane * VT + 1040) = z;
        }
        BAR_LGKM();
        {
            const int T0 = rb * 8 + 2 * hw;
            const u32x4 k0 = *(const LDSP u32x4*)(sm + L_K + T0 * 1024 + lane * 16), k1 = *(const LDSP u32x4*)(sm + L_K + (T0 + 1) * 1024 + lane * 16);
            const u32x4 v0 = *(const LDSP u32x4*)(sm + L_V + T0 * VT + lane * 16), v1 = *(const LDSP u32x4*)(sm + L_V + (T0 + 1) * VT + lane * 16);
            const float km = *(const LDSP float*)(sm + L_QX + 8192 + (lane & 7) * 4);
            char* kg = ws + WS_K + ((size_t)head * 64 + T0) * 1024;
            char* vg = ws + WS_V + ((size_t)head * 64 + T0) * 1024;
            store16_dev(kg, lane * 16, k0); store16_dev(kg + 1024, lane * 16, k1);
            store16_dev(vg, lane * 16, v0); store16_dev(vg + 1024, lane * 16, v1);
            if (hw == 0 && lane < 8) store4_dev(ws + WS_KM, (head * 64 + rb * 8 + lane) * 4, km);
        }
        BAR_LGKM();
        {
            const int j0 = 8 * ht;
            int ci[9];
#pragma unroll
            for (int e = 0; e < 9; ++e) { const int j = j0 - 1 + e; const int bits = (int)(cvtpk_bf16((float)j, 0.f) & 0xffffu); ci[e] = tab_idx(j < 128 ? (j < 0 ? 0 : j) : bits - 17024); }
#pragma unroll
            for (int cc = 0; cc < 2; ++cc) {
                const LDSP float* ta = (const LDSP float*)(sm + L_TAB + (2 * cc) * TAB_SZ);
                const LDSP float* tb = (const LDSP float*)(sm + L_TAB + (2 * cc + 1) * TAB_SZ);
                float k[9];
#pragma unroll
                for (int e = 0; e < 9; ++e) k[e] = ta[ci[e]] + tb[ci[e]];
                if (ht == 0) k[0] = 0.f;
                const u32x4 c0 = {cvtpk_bf16(k[1], k[2]), cvtpk_bf16(k[3], k[4]), cvtpk_bf16(k[5], k[6]), cvtpk_bf16(k[7], k[8])};
                const u32x4 c1 = {cvtpk_bf16(k[0], k[1]), cvtpk_bf16(k[2], k[3]), cvtpk_bf16(k[4], k[5]), cvtpk_bf16(k[6], k[7])};
                *(LDSP u32x4*)(sm + L_TAP + cc * TAP_CH + (ht + 8) * 16) = c0;
                *(LDSP u32x4*)(sm + L_TAP + cc * TAP_CH + TAP_C1 + (ht + 8) * 16) = c1;
            }
            if (ht < 32) { const u32x4 z = {0, 0, 0, 0}; *(LDSP u32x4*)(sm + L_TAP + (ht >> 4) * TAP_CH + ((ht >> 3) & 1) * TAP_C1 + (ht & 7) * 16) = z; }
        }
        WAIT_VM(0);
        if (lane == 0 && __hip_atomic_fetch_add(cnt + 1, 1u, __ATOMIC_RELAXED, __HIP_MEMORY_SCOPE_WORKGROUP) == 3)
            __hip_atomic_store((gu64*)(ws + WS_F1 + wg * 128), tag, RLX_AGENT);
        STAMP(5);
        auto fetch_issue = [&](int j, float& kmv) {
            const int pc = (rb + j) & 7;
            wait_tags((const gu64*)(ws + WS_F1 + (8 * head + pc) * 128), tag);
            const char* kg = ws + WS_K + ((size_t)head * 64 + 8 * pc) * 1024;
            const char* vg = ws + WS_V + ((size_t)head * 64 + 8 * pc) * 1024;
#pragma unroll
            for (int i = 0; i < 8; ++i) {
                glds16_dev(kg + i * 1024, lane * 16, lds0 + L_K + (8 * pc + i) * 1024);
                glds16_dev(vg + i * 1024, lane * 16, lds0 + L_V + (8 * pc + i) * VT);
            }
            asm volatile("global_load_dword %0, %1, %2 sc1" : "=&v"(kmv) : "v"((lane & 7) * 4), "s"(ws + WS_KM + (head * 64 + 8 * pc) * 4) : "memory");
        };
        auto fetch_done = [&](int j, float& kmv) {
            asm volatile("" : "+v"(kmv));
            const unsigned kb = __float_as_uint(wave_max(kmv));
            if (lane == 0) *(LDSP volatile unsigned*)(sm + L_FLAG + 4 * j) = kb > 1u ? kb : 1u;
        };
        float km1, km2 = 0.f;
        fetch_issue(hw + 1, km1);
        STAMP(12);
        WAIT_VM(0); fetch_done(hw + 1, km1);
        STAMP(13);
        if (hw < 3) fetch_issue(hw + 5, km2);
        unsigned ur[8] = {};
#pragma unroll
        for (int i = 0; i < 8; ++i) asm volatile("global_load_dword %0, %1, %2" : "=&v"(ur[i]) : "v"((unsigned)(512 * hw + 64 * i + lane) * 1024), "s"((const char*)u + cp * 4) : "memory");
        if (hw < 3) { WAIT_VM(8); fetch_done(hw + 5, km2); }
        helper_sync(3);
        asm volatile("s_waitcnt vmcnt(0)" : "+v"(ur[0]), "+v"(ur[1]), "+v"(ur[2]), "+v"(ur[3]), "+v"(ur[4]), "+v"(ur[5]), "+v"(ur[6]), "+v"(ur[7]) :: "memory");
        { const u32x4 z = {0, 0, 0, 0};
          for (int i = ht; i < 31 * 9; i += 256) *(LDSP u32x4*)(sm + L_Z + i * 16) = z; }
        {
            const unsigned pos = 2 * (lane ^ 7);
#pragma unroll
            for (int i = 0; i < 8; ++i) {
                const int blk = 8 * hw + i;
                *(LDSP unsigned short*)(sm + L_U0 + blk * UBLK + pos) = (unsigned short)(ur[i] & 0xffffu);
                *(LDSP unsigned short*)(sm + L_Z - (blk + 1) * UBLK + pos) = (unsigned short)(ur[i] >> 16);
            }
        }
        WAIT_LGKM();
        if (lane == 0) __hip_atomic_fetch_add(cnt, 1u, __ATOMIC_RELAXED, __HIP_MEMORY_SCOPE_WORKGROUP);
        STAMP(6);
        STAMP(7);
        wait_lds_word((const LDSP volatile unsigned*)cnt, 4);
#define CONV_PRIO 1
        __builtin_amdgcn_s_setprio(CONV_PRIO);
        STAMP(8);
        {
            const int rho = hw & 1;
            const int e0 = 32 * rho + r - 8 * h - 7 + 64;
            const LDSP char* ap = sm + L_TAP + c * TAP_CH + ((r & 1) ? 2 * e0 : TAP_C1 + 2 * (e0 + 1)) - 96;
            const int ustep = c ? (int)UBLK : -(int)UBLK;
            const LDSP char* bp = sm + (c ? L_Z - (r + 1) * UBLK : L_U0 + r * UBLK) + 16 * h;
            f32x16 y = {};
#pragma unroll 2
            for (int m = 0; m < 32; ++m) {
#pragma unroll
                for (int k = 0; k < 4; ++k) {
                    const u32x4a a = *(const LDSP u32x4a*)(ap + 96 - 32 * k);
                    const bf16x8 bb = *(const LDSP bf16x8*)(bp + 32 * k);
                    y = mfma_bf16(as_bf((u32x4)a), bb, y);
                }
                ap += 128; bp += ustep;
            }
            STAMP(9);
            const LDSP char* up = sm + (c ? L_Z - (r + 1) * UBLK : L_U0 + r * UBLK) + 64 * rho + 8 * (1 - h);
            unsigned short* op = out + (size_t)(64 * r + 32 * rho + 4 * h) * 1024 + ch;
#pragma unroll
            for (int gq = 0; gq < 4; ++gq) {
                const u32x2 uu = *(const LDSP u32x2*)(up + 16 * gq);
#pragma unroll
                for (int e = 0; e < 4; ++e) {
                    const unsigned pr = uu[(3 - e) >> 1];
                    const float uf = __uint_as_float(((3 - e) & 1) ? (pr & 0xffff0000u) : (pr << 16));
                    const float res = bf16r(y[4 * gq + e]) + bf16r(uf * dsk);
                    op[(size_t)(8 * gq + e) * 1024] = (unsigned short)(cvtpk_bf16(res, res) & 0xffffu);
                }
            }
        }
        STAMP(10);
        BAR_LGKM();
        {
            const int fg = head & 3;
            const char* wg_ = (const char*)wo + fg * 256 + hw * 64;
            const unsigned woff = (lane >> 2) * 1024 + (lane & 3) * 16;
#pragma unroll 4
            for (int i = 0; i < 32; ++i) glds16(wg_ + (size_t)i * 16384, woff, lds0 + L3_W + hw * 32768 + i * 1024);
        }
        asm volatile("s_barrier" ::: "memory");
        asm volatile("s_barrier" ::: "memory");
        asm volatile("s_barrier" ::: "memory");
        WAIT_BAR(0);
        STAMP(11);
    }
    STAMP_OUT(ws);
}

extern "C" void kernel_launch(void* const* d_in, const int* in_sizes, int n_in, void* d_out, int out_size, void* d_ws, size_t ws_size,
                              hipStream_t stream) {
    kf_fused<<<256, 768, 0, stream>>>((const unsigned short*)d_in[0], (const unsigned short*)d_in[1], (const unsigned short*)d_in[2],
                                      (const unsigned short*)d_in[3], (const unsigned short*)d_in[4], (const unsigned short*)d_in[5],
                                      (const unsigned short*)d_in[6], (const unsigned short*)d_in[7], (const unsigned short*)d_in[8],
                                      (const unsigned short*)d_in[9], (char*)d_ws, (unsigned short*)d_out);
}
```

```cpp
#include <hip/hip_runtime.h>
#include <hip/hip_bf16.h>
#include <stdint.h>

typedef __bf16 bf16x8 __attribute__((ext_vector_type(8)));
typedef __bf16 bf16x2 __attribute__((ext_vector_type(2)));
typedef _Float16 f16x8 __attribute__((ext_vector_type(8)));
typedef _Float16 f16x2 __attribute__((ext_vector_type(2)));
typedef float f32x16 __attribute__((ext_vector_type(16)));
typedef float f32x2 __attribute__((ext_vector_type(2)));
typedef float f32x4 __attribute__((ext_vector_type(4)));
typedef __bf16 bf16x16 __attribute__((ext_vector_type(16)));
typedef unsigned u32x4 __attribute__((ext_vector_type(4)));
typedef unsigned u32x2 __attribute__((ext_vector_type(2)));
typedef unsigned u32x8 __attribute__((ext_vector_type(8)));
typedef unsigned u32x16 __attribute__((ext_vector_type(16)));
typedef short s16x4 __attribute__((ext_vector_type(4)));
typedef __attribute__((address_space(1))) unsigned long long gu64;
#define LDSP __attribute__((address_space(3)))
#define DI __device__ __forceinline__
#define RLX_AGENT __ATOMIC_RELAXED, __HIP_MEMORY_SCOPE_AGENT

constexpr size_t WS_K = 0;
constexpr size_t WS_V = WS_K + (size_t)32 * 65536;
constexpr size_t WS_O = WS_V + (size_t)32 * 65536;
constexpr size_t WS_KM = WS_O + (size_t)64 * 32768;
constexpr size_t WS_UT = WS_KM + 32 * 64 * 4;
constexpr size_t WS_YT = WS_UT + (size_t)512 * 5120;
constexpr size_t WS_F0 = WS_YT + (size_t)512 * 4096;
constexpr size_t WS_F1 = WS_F0 + 256 * 128;
constexpr size_t WS_F2 = WS_F1 + 256 * 128;
constexpr size_t WS_F3 = WS_F2 + 256 * 128;
constexpr size_t WS_EP = WS_F3 + 256 * 128;
constexpr unsigned VT = 1024;
constexpr unsigned TAP_C1 = 4288, TAP_CH = 8512, TAP_USED = 17024, UBLK = 80;
constexpr unsigned L_K = 0, L_V = 65536, L_TAP = 133120, L_U1 = L_TAP + TAP_USED, L_Z = L_U1 + 64 * UBLK,
                   L_U0 = L_Z + 31 * UBLK, L_END = L_U0 + 64 * UBLK;
constexpr unsigned L_FLAG = L_TAP + TAP_CH + 4224;
constexpr unsigned STAGE = 38912, ST_W = 32768, ST_WV = ST_W + 4096, L_UST = 4 * STAGE;
constexpr unsigned P_ROW = 272, P_BUF = 16 * P_ROW;
constexpr unsigned TAB_SZ = 2896;
constexpr unsigned L_QX = 163840 - 8256;
constexpr unsigned L3_O = 0, L_PART = 131072, PART_SZ = 3072, L_OCNT = L_PART + 8 * PART_SZ;
constexpr unsigned LDS_BYTES = 163840;
constexpr float QSCALE = 0.25f * 1.4426950408889634f;
constexpr float SAFE_BOUND = 40.0f;
constexpr unsigned SPIN_CAP = 1u << 14, SPIN_CAP_LDS = 1u << 17;

DI float bf2f(unsigned short v) { return __uint_as_float((unsigned)v << 16); }
DI unsigned cvtpk_bf16(float lo, float hi) { f32x2 v = {lo, hi}; return __builtin_bit_cast(unsigned, __builtin_convertvector(v, bf16x2)); }
DI unsigned cvtpk_f16(float lo, float hi) { f32x2 v = {lo, hi}; return __builtin_bit_cast(unsigned, __builtin_convertvector(v, f16x2)); }
DI float bf16r(float x) { return __uint_as_float(cvtpk_bf16(x, x) << 16); }
DI f32x16 mfma_bf16(bf16x8 a, bf16x8 b, f32x16 c) { return __builtin_amdgcn_mfma_f32_32x32x16_bf16(a, b, c, 0, 0, 0); }
DI f32x16 mfma_f16(f16x8 a, f16x8 b, f32x16 c) { return __builtin_amdgcn_mfma_f32_32x32x16_f16(a, b, c, 0, 0, 0); }
DI bf16x8 as_bf(u32x4 v) { return __builtin_bit_cast(bf16x8, v); }
DI void glds16(const void* sbase, unsigned voff, unsigned lds) {
    unsigned sv;
    asm volatile("s_mov_b32 %0, m0\n\ts_mov_b32 m0, %3\n\ts_nop 0\n\tglobal_load_lds_dwordx4 %1, %2\n\ts_mov_b32 m0, %0"
                 : "=&s"(sv) : "v"(voff), "s"(sbase), "s"(lds) : "memory");
}
DI void glds16_dev(const void* sbase, unsigned voff, unsigned lds) {
    unsigned sv;
    asm volatile("s_mov_b32 %0, m0\n\ts_mov_b32 m0, %3\n\ts_nop 0\n\tglobal_load_lds_dwordx4 %1, %2 sc1\n\ts_mov_b32 m0, %0"
                 : "=&s"(sv) : "v"(voff), "s"(sbase), "s"(lds) : "memory");
}
DI void store16_dev(void* sbase, unsigned voff, u32x4 v) {
    asm volatile("global_store_dwordx4 %0, %1, %2 sc1\n\ts_nop 1" :: "v"(voff), "v"(v), "s"(sbase) : "memory");
}
DI void store16_dev_v(void* p, u32x4 v) {
    asm volatile("global_store_dwordx4 %0, %1, off sc1\n\ts_nop 1" :: "v"(p), "v"(v) : "memory");
}
DI void store4_dev(void* sbase, unsigned voff, float v) {
    asm volatile("global_store_dword %0, %1, %2 sc1" :: "v"(voff), "v"(v), "s"(sbase) : "memory");
}
#define WAIT_VM(n) asm volatile("s_waitcnt vmcnt(" #n ")" ::: "memory")
#define WAIT_LGKM() asm volatile("s_waitcnt lgkmcnt(0)" ::: "memory")
#define WAIT_BAR(n) asm volatile("s_waitcnt vmcnt(" #n ") lgkmcnt(0)\n\ts_barrier" ::: "memory")
#define BAR_LGKM() asm volatile("s_waitcnt lgkmcnt(0)\n\ts_barrier" ::: "memory")
template <int CTRL> DI float dpp_max(float v) {
    const int o = __builtin_amdgcn_update_dpp(__float_as_int(v), __float_as_int(v), CTRL, 0xf, 0xf, false);
    return fmaxf(v, __int_as_float(o));
}
DI float wave_max(float v) {
    v = dpp_max<0x111>(v); v = dpp_max<0x112>(v); v = dpp_max<0x114>(v); v = dpp_max<0x118>(v);
    v = dpp_max<0x142>(v); v = dpp_max<0x143>(v);
    return __int_as_float(__builtin_amdgcn_readlane(__float_as_int(v), 63));
}
DI void wave_max2(float& a, float& b) {
#define WM2(ctl) "v_max_f32_dpp %0, %0, %0 " ctl " row_mask:0xf bank_mask:0xf\n\tv_max_f32_dpp %1, %1, %1 " ctl " row_mask:0xf bank_mask:0xf\n\ts_nop 0\n\t"
    asm volatile("s_nop 1\n\t" WM2("row_shr:1") WM2("row_shr:2") WM2("row_shr:4") WM2("row_shr:8") WM2("row_bcast:15") WM2("row_bcast:31") : "+v"(a), "+v"(b));
#undef WM2
    a = __int_as_float(__builtin_amdgcn_readlane(__float_as_int(a), 63));
    b = __int_as_float(__builtin_amdgcn_readlane(__float_as_int(b), 63));
}
DI float max3f(float a, float b, float c) { float r; asm("v_max3_f32 %0, %1, %2, %3" : "=v"(r) : "v"(a), "v"(b), "v"(c)); return r; }
DI float max3abs(float a, float b, float c) { float r; asm("v_max3_f32 %0, |%1|, |%2|, %3" : "=v"(r) : "v"(a), "v"(b), "v"(c)); return r; }
DI float half_sum(float v) {
    auto r = __builtin_amdgcn_permlane32_swap(__float_as_uint(v), __float_as_uint(v), false, false);
    return __uint_as_float(r[0]) + __uint_as_float(r[1]);
}
DI float half_max(float v) {
    auto r = __builtin_amdgcn_permlane32_swap(__float_as_uint(v), __float_as_uint(v), false, false);
    return fmaxf(__uint_as_float(r[0]), __uint_as_float(r[1]));
}
DI u32x4 pack8(const f32x16& a, int o, float s) {
    u32x4 r;
    r[0] = cvtpk_bf16(a[o + 0] * s, a[o + 1] * s); r[1] = cvtpk_bf16(a[o + 2] * s, a[o + 3] * s);
    r[2] = cvtpk_bf16(a[o + 4] * s, a[o + 5] * s); r[3] = cvtpk_bf16(a[o + 6] * s, a[o + 7] * s);
    return r;
}
DI bf16x8 tr_frag(const LDSP char* p, int off_lo, int off_hi) {
    const s16x4 lo = __builtin_amdgcn_ds_read_tr16_b64_v4i16((LDSP s16x4*)(p + off_lo));
    const s16x4 hi = __builtin_amdgcn_ds_read_tr16_b64_v4i16((LDSP s16x4*)(p + off_hi));
    typedef short s16x8 __attribute__((ext_vector_type(8)));
    const s16x8 v = __builtin_shufflevector(lo, hi, 0, 1, 2, 3, 4, 5, 6, 7);
    return __builtin_bit_cast(bf16x8, v);
}
DI void wait_tags(const gu64* p, unsigned long long tag) {
    for (unsigned spins = 0; spins < SPIN_CAP; ++spins) {
        const unsigned long long x = __hip_atomic_load(p, RLX_AGENT);
        if (__all(x == tag)) break;
        __builtin_amdgcn_s_sleep(1);
    }
    asm volatile("" ::: "memory");
}
DI unsigned wait_lds_word(const LDSP volatile unsigned* p, unsigned atleast) {
    unsigned v = 0;
    for (unsigned spins = 0; spins < SPIN_CAP_LDS; ++spins) {
        v = __builtin_amdgcn_readfirstlane(*p);
        if (v >= atleast) break;
        __builtin_amdgcn_s_sleep(2);
    }
    asm volatile("" ::: "memory");
    return v;
}
struct cplx { float r, i; };
DI float fmul1(float a, float b) { float r; asm("v_mul_f32 %0, %1, %2" : "=v"(r) : "v"(a), "v"(b)); return r; }
DI float ffma1(float a, float b, float c) { float r; asm("v_fma_f32 %0, %1, %2, %3" : "=v"(r) : "v"(a), "v"(b), "v"(c)); return r; }
DI float fnma1(float a, float b, float c) { float r; asm("v_fma_f32 %0, -%1, %2, %3" : "=v"(r) : "v"(a), "v"(b), "v"(c)); return r; }
DI cplx cmul(cplx a, cplx b) { cplx o; o.r = fnma1(a.i, b.i, fmul1(a.r, b.r)); o.i = ffma1(a.i, b.r, fmul1(a.r, b.i)); return o; }
DI cplx zpow(float xl, float yr, float p) {
    const float e = __builtin_amdgcn_exp2f(xl * p);
    const float th = __builtin_amdgcn_fractf(yr * p);
    cplx o; o.r = e * __builtin_amdgcn_cosf(th); o.i = e * __builtin_amdgcn_sinf(th); return o;
}
DI int tab_idx(int ci) { return ci + (ci >> 3); }

#define ATTN_ASM_OWN_A \
    "v_mov_b32 v44, 0\n\t" \
    "v_mov_b32 v45, 0\n\t" \
    "v_mov_b32 v46, 0\n\t" \
    "v_mov_b32 v47, 0\n\t" \
    "v_mov_b32 v52, 0\n\t" \
    "v_mov_b32 v53, 0\n\t" \
    "v_mov_b32 v54, 0\n\t" \
    "v_mov_b32 v55, 0\n\t" \
    "v_mov_b32 v56, 0\n\t" \
    "v_mov_b32 v57, 0\n\t" \
    "v_mov_b32 v58, 0\n\t" \
    "v_mov_b32 v59, 0\n\t" \
    "v_mov_b32 v92, 0\n\t" \
    "v_mov_b32 v93, 0\n\t" \
    "v_mov_b32 v94, 0\n\t" \
    "v_mov_b32 v95, 0\n\t" \
    "v_mov_b32 v96, 0\n\t" \
    "v_mov_b32 v97, 0\n\t" \
    "v_mov_b32 v98, 0\n\t" \
    "v_mov_b32 v99, 0\n\t" \
    "v_mov_b32 v100, 0\n\t" \
    "v_mov_b32 v101, 0\n\t" \
    "v_mov_b32 v102, 0\n\t" \
    "v_mov_b32 v103, 0\n\t" \
    "v_mov_b32 v104, 0\n\t" \
    "v_mov_b32 v105, 0\n\t" \
    "v_mov_b32 v106, 0\n\t" \
    "v_mov_b32 v107, 0\n\t" \
    "v_mov_b32 v108, 0\n\t" \
    "v_mov_b32 v109, 0\n\t" \
    "v_mov_b32 v110, 0\n\t" \
    "v_mov_b32 v111, 0\n\t" \
    "v_mov_b32 v112, 0\n\t" \
    "v_mov_b32 v113, 0\n\t" \
    "v_mov_b32 v114, 0\n\t" \
    "v_mov_b32 v115, 0\n\t" \
    "v_mov_b32 v116, 0\n\t" \
    "v_mov_b32 v117, 0\n\t" \
    "v_mov_b32 v118, 0\n\t" \
    "v_mov_b32 v119, 0\n\t" \
    "v_mov_b32 v120, 0\n\t" \
    "v_mov_b32 v121, 0\n\t" \
    "v_mov_b32 v122, 0\n\t" \
    "v_mov_b32 v123, 0\n\t" \
    "v_mov_b32 v138, 0\n\t" \
    "v_mov_b32 v139, 0\n\t" \
    "v_mov_b32 v140, 0\n\t" \
    "v_mov_b32 v141, 0\n\t" \
    "v_mov_b32 v142, 0\n\t" \
    "v_mov_b32 v143, 0\n\t" \
    "v_mov_b32 v144, 0\n\t" \
    "v_mov_b32 v145, 0\n\t" \
    "ds_read_b128 v[40:43], v124 offset:0\n\t" \
    "s_waitcnt lgkmcnt(0)\n\t" \
    "v_mfma_f32_32x32x16_bf16 v[60:75], v[40:43], v[32:35], 0\n\t" \
    "s_nop 7\n\t" \
    "s_nop 3\n\t" \
    "v_exp_f32 v60, v60\n\t" \
    "v_exp_f32 v61, v61\n\t" \
    "v_smfmac_f32_32x32x32_bf16 v[108:123], v[44:47], v[52:59], v126\n\t" \
    "ds_read_b128 v[44:47], v125 offset:0\n\t" \
    "v_exp_f32 v62, v62\n\t" \
    "v_exp_f32 v63, v63\n\t" \
    "v_exp_f32 v64, v64\n\t" \
    "v_exp_f32 v65, v65\n\t" \
    "v_smfmac_f32_16x16x64_bf16 v[142:145], v[48:51], v[52:59], v137\n\t" \
    "v_exp_f32 v66, v66\n\t" \
    "v_exp_f32 v67, v67\n\t" \
    "v_cvt_pk_bf16_f32 v52, v60, v61\n\t" \
    "v_cvt_pk_bf16_f32 v53, v62, v63\n\t" \
    "v_cvt_pk_bf16_f32 v54, v64, v65\n\t" \
    "v_cvt_pk_bf16_f32 v55, v66, v67\n\t" \
    "v_mfma_f32_32x32x16_bf16 v[76:91], v[40:43], v[36:39], 0\n\t" \
    "ds_read_b128 v[40:43], v124 offset:1024\n\t" \
    "v_exp_f32 v68, v68\n\t" \
    "v_exp_f32 v69, v69\n\t" \
    "v_exp_f32 v70, v70\n\t" \
    "v_exp_f32 v71, v71\n\t" \
    "v_exp_f32 v72, v72\n\t" \
    "v_exp_f32 v73, v73\n\t" \
    "v_exp_f32 v74, v74\n\t" \
    "v_exp_f32 v75, v75\n\t" \
    "v_cvt_pk_bf16_f32 v56, v68, v69\n\t" \
    "v_cvt_pk_bf16_f32 v57, v70, v71\n\t" \
    "v_cvt_pk_bf16_f32 v58, v72, v73\n\t" \
    "v_cvt_pk_bf16_f32 v59, v74, v75\n\t" \
    "v_exp_f32 v76, v76\n\t" \
    "v_exp_f32 v77, v77\n\t" \
    "s_waitcnt lgkmcnt(0)\n\t" \
    "v_smfmac_f32_32x32x32_bf16 v[92:107], v[44:47], v[52:59], v126\n\t" \
    "v_exp_f32 v78, v78\n\t" \
    "v_exp_f32 v79, v79\n\t" \
    "v_exp_f32 v80, v80\n\t" \
    "v_exp_f32 v81, v81\n\t" \
    "v_smfmac_f32_16x16x64_bf16 v[138:141], v[48:51], v[52:59], v137\n\t" \
    "v_exp_f32 v82, v82\n\t" \
    "v_exp_f32 v83, v83\n\t" \
    "v_cvt_pk_bf16_f32 v52, v76, v77\n\t" \
    "v_cvt_pk_bf16_f32 v53, v78, v79\n\t" \
    "v_cvt_pk_bf16_f32 v54, v80, v81\n\t" \
    "v_cvt_pk_bf16_f32 v55, v82, v83\n\t" \
    "v_mfma_f32_32x32x16_bf16 v[60:75], v[40:43], v[32:35], 0\n\t" \
    "v_exp_f32 v84, v84\n\t" \
    "v_exp_f32 v85, v85\n\t" \
    "v_exp_f32 v86, v86\n\t" \
    "v_exp_f32 v87, v87\n\t" \
    "v_exp_f32 v88, v88\n\t" \
    "v_exp_f32 v89, v89\n\t" \
    "v_exp_f32 v90, v90\n\t" \
    "v_exp_f32 v91, v91\n\t" \
    "v_cvt_pk_bf16_f32 v56, v84, v85\n\t" \
    "v_cvt_pk_bf16_f32 v57, v86, v87\n\t" \
    "v_cvt_pk_bf16_f32 v58, v88, v89\n\t" \
    "v_cvt_pk_bf16_f32 v59, v90, v91\n\t" \
    "v_exp_f32 v60, v60\n\t" \
    "v_exp_f32 v61, v61\n\t" \
    "v_smfmac_f32_32x32x32_bf16 v[108:123], v[44:47], v[52:59], v126\n\t" \
    "ds_read_b128 v[44:47], v125 offset:1024\n\t" \
    "v_exp_f32 v62, v62\n\t" \
    "v_exp_f32 v63, v63\n\t" \
    "v_exp_f32 v64, v64\n\t" \
    "v_exp_f32 v65, v65\n\t" \
    "v_smfmac_f32_16x16x64_bf16 v[142:145], v[48:51], v[52:59], v137\n\t" \
    "v_exp_f32 v66, v66\n\t" \
    "v_exp_f32 v67, v67\n\t" \
    "v_cvt_pk_bf16_f32 v52, v60, v61\n\t" \
    "v_cvt_pk_bf16_f32 v53, v62, v63\n\t" \
    "v_cvt_pk_bf16_f32 v54, v64, v65\n\t" \
    "v_cvt_pk_bf16_f32 v55, v66, v67\n\t" \
    "v_mfma_f32_32x32x16_bf16 v[76:91], v[40:43], v[36:39], 0\n\t" \
    "ds_read_b128 v[40:43], v124 offset:2048\n\t" \
    "v_exp_f32 v68, v68\n\t" \
    "v_exp_f32 v69, v69\n\t" \
    "v_exp_f32 v70, v70\n\t" \
    "v_exp_f32 v71, v71\n\t" \
    "v_exp_f32 v72, v72\n\t" \
    "v_exp_f32 v73, v73\n\t" \
    "v_exp_f32 v74, v74\n\t" \
    "v_exp_f32 v75, v75\n\t" \
    "v_cvt_pk_bf16_f32 v56, v68, v69\n\t" \
    "v_cvt_pk_bf16_f32 v57, v70, v71\n\t" \
    "v_cvt_pk_bf16_f32 v58, v72, v73\n\t" \
    "v_cvt_pk_bf16_f32 v59, v74, v75\n\t" \
    "v_exp_f32 v76, v76\n\t" \
    "v_exp_f32 v77, v77\n\t" \
    "s_waitcnt lgkmcnt(0)\n\t" \
    "v_smfmac_f32_32x32x32_bf16 v[92:107], v[44:47], v[52:59], v126\n\t" \
    "v_exp_f32 v78, v78\n\t" \
    "v_exp_f32 v79, v79\n\t" \
    "v_exp_f32 v80, v80\n\t" \
    "v_exp_f32 v81, v81\n\t" \
    "v_smfmac_f32_16x16x64_bf16 v[138:141], v[48:51], v[52:59], v137\n\t" \
    "v_exp_f32 v82, v82\n\t" \
    "v_exp_f32 v83, v83\n\t" \
    "v_cvt_pk_bf16_f32 v52, v76, v77\n\t" \
    "v_cvt_pk_bf16_f32 v53, v78, v79\n\t" \
    "v_cvt_pk_bf16_f32 v54, v80, v81\n\t" \
    "v_cvt_pk_bf16_f32 v55, v82, v83\n\t" \
    "v_mfma_f32_32x32x16_bf16 v[60:75], v[40:43], v[32:35], 0\n\t" \
    "v_exp_f32 v84, v84\n\t" \
    "v_exp_f32 v85, v85\n\t" \
    "v_exp_f32 v86, v86\n\t" \
    "v_exp_f32 v87, v87\n\t" \
    "v_exp_f32 v88, v88\n\t" \
    "v_exp_f32 v89, v89\n\t" \
    "v_exp_f32 v90, v90\n\t" \
    "v_exp_f32 v91, v91\n\t" \
    "v_cvt_pk_bf16_f32 v56, v84, v85\n\t" \
    "v_cvt_pk_bf16_f32 v57, v86, v87\n\t" \
    "v_cvt_pk_bf16_f32 v58, v88, v89\n\t" \
    "v_cvt_pk_bf16_f32 v59, v90, v91\n\t" \
    "s_waitcnt lgkmcnt(0)\n\t" \
    "s_nop 15\n\t" \
    "s_nop 3\n\t"

#define ATTN_ASM_OWN_B \
    "v_exp_f32 v60, v60\n\t" \
    "v_exp_f32 v61, v61\n\t" \
    "v_smfmac_f32_32x32x32_bf16 v[108:123], v[44:47], v[52:59], v126\n\t" \
    "ds_read_b128 v[44:47], v125 offset:2048\n\t" \
    "v_exp_f32 v62, v62\n\t" \
    "v_exp_f32 v63, v63\n\t" \
    "v_exp_f32 v64, v64\n\t" \
    "v_exp_f32 v65, v65\n\t" \
    "v_smfmac_f32_16x16x64_bf16 v[142:145], v[48:51], v[52:59], v137\n\t" \
    "v_exp_f32 v66, v66\n\t" \
    "v_exp_f32 v67, v67\n\t" \
    "v_cvt_pk_bf16_f32 v52, v60, v61\n\t" \
    "v_cvt_pk_bf16_f32 v53, v62, v63\n\t" \
    "v_cvt_pk_bf16_f32 v54, v64, v65\n\t" \
    "v_cvt_pk_bf16_f32 v55, v66, v67\n\t" \
    "v_mfma_f32_32x32x16_bf16 v[76:91], v[40:43], v[36:39], 0\n\t" \
    "ds_read_b128 v[40:43], v124 offset:3072\n\t" \
    "v_exp_f32 v68, v68\n\t" \
    "v_exp_f32 v69, v69\n\t" \
    "v_exp_f32 v70, v70\n\t" \
    "v_exp_f32 v71, v71\n\t" \
    "v_exp_f32 v72, v72\n\t" \
    "v_exp_f32 v73, v73\n\t" \
    "v_exp_f32 v74, v74\n\t" \
    "v_exp_f32 v75, v75\n\t" \
    "v_cvt_pk_bf16_f32 v56, v68, v69\n\t" \
    "v_cvt_pk_bf16_f32 v57, v70, v71\n\t" \
    "v_cvt_pk_bf16_f32 v58, v72, v73\n\t" \
    "v_cvt_pk_bf16_f32 v59, v74, v75\n\t" \
    "v_exp_f32 v76, v76\n\t" \
    "v_exp_f32 v77, v77\n\t" \
    "s_waitcnt lgkmcnt(0)\n\t" \
    "v_smfmac_f32_32x32x32_bf16 v[92:107], v[44:47], v[52:59], v126\n\t" \
    "v_exp_f32 v78, v78\n\t" \
    "v_exp_f32 v79, v79\n\t" \
    "v_exp_f32 v80, v80\n\t" \
    "v_exp_f32 v81, v81\n\t" \
    "v_smfmac_f32_16x16x64_bf16 v[138:141], v[48:51], v[52:59], v137\n\t" \
    "v_exp_f32 v82, v82\n\t" \
    "v_exp_f32 v83, v83\n\t" \
    "v_cvt_pk_bf16_f32 v52, v76, v77\n\t" \
    "v_cvt_pk_bf16_f32 v53, v78, v79\n\t" \
    "v_cvt_pk_bf16_f32 v54, v80, v81\n\t" \
    "v_cvt_pk_bf16_f32 v55, v82, v83\n\t" \
    "v_mfma_f32_32x32x16_bf16 v[60:75], v[40:43], v[32:35], 0\n\t" \
    "v_exp_f32 v84, v84\n\t" \
    "v_exp_f32 v85, v85\n\t" \
    "v_exp_f32 v86, v86\n\t" \
    "v_exp_f32 v87, v87\n\t" \
    "v_exp_f32 v88, v88\n\t" \
    "v_exp_f32 v89, v89\n\t" \
    "v_exp_f32 v90, v90\n\t" \
    "v_exp_f32 v91, v91\n\t" \
    "v_cvt_pk_bf16_f32 v56, v84, v85\n\t" \
    "v_cvt_pk_bf16_f32 v57, v86, v87\n\t" \
    "v_cvt_pk_bf16_f32 v58, v88, v89\n\t" \
    "v_cvt_pk_bf16_f32 v59, v90, v91\n\t" \
    "v_exp_f32 v60, v60\n\t" \
    "v_exp_f32 v61, v61\n\t" \
    "v_smfmac_f32_32x32x32_bf16 v[108:123], v[44:47], v[52:59], v126\n\t" \
    "ds_read_b128 v[44:47], v125 offset:3072\n\t" \
    "v_exp_f32 v62, v62\n\t" \
    "v_exp_f32 v63, v63\n\t" \
    "v_exp_f32 v64, v64\n\t" \
    "v_exp_f32 v65, v65\n\t" \
    "v_smfmac_f32_16x16x64_bf16 v[142:145], v[48:51], v[52:59], v137\n\t" \
    "v_exp_f32 v66, v66\n\t" \
    "v_exp_f32 v67, v67\n\t" \
    "v_cvt_pk_bf16_f32 v52, v60, v61\n\t" \
    "v_cvt_pk_bf16_f32 v53, v62, v63\n\t" \
    "v_cvt_pk_bf16_f32 v54, v64, v65\n\t" \
    "v_cvt_pk_bf16_f32 v55, v66, v67\n\t" \
    "v_mfma_f32_32x32x16_bf16 v[76:91], v[40:43], v[36:39], 0\n\t" \
    "ds_read_b128 v[40:43], v124 offset:0\n\t" \
    "v_exp_f32 v68, v68\n\t" \
    "v_exp_f32 v69, v69\n\t" \
    "v_exp_f32 v70, v70\n\t" \
    "v_exp_f32 v71, v71\n\t" \
    "v_exp_f32 v72, v72\n\t" \
    "v_exp_f32 v73, v73\n\t" \
    "v_exp_f32 v74, v74\n\t" \
    "v_exp_f32 v75, v75\n\t" \
    "v_cvt_pk_bf16_f32 v56, v68, v69\n\t" \
    "v_cvt_pk_bf16_f32 v57, v70, v71\n\t" \
    "v_cvt_pk_bf16_f32 v58, v72, v73\n\t" \
    "v_cvt_pk_bf16_f32 v59, v74, v75\n\t" \
    "v_exp_f32 v76, v76\n\t" \
    "v_exp_f32 v77, v77\n\t" \
    "s_waitcnt lgkmcnt(0)\n\t" \
    "v_smfmac_f32_32x32x32_bf16 v[92:107], v[44:47], v[52:59], v126\n\t" \
    "v_exp_f32 v78, v78\n\t" \
    "v_exp_f32 v79, v79\n\t" \
    "v_exp_f32 v80, v80\n\t" \
    "v_exp_f32 v81, v81\n\t" \
    "v_smfmac_f32_16x16x64_bf16 v[138:141], v[48:51], v[52:59], v137\n\t" \
    "v_exp_f32 v82, v82\n\t" \
    "v_exp_f32 v83, v83\n\t" \
    "v_cvt_pk_bf16_f32 v52, v76, v77\n\t" \
    "v_cvt_pk_bf16_f32 v53, v78, v79\n\t" \
    "v_cvt_pk_bf16_f32 v54, v80, v81\n\t" \
    "v_cvt_pk_bf16_f32 v55, v82, v83\n\t" \
    "v_mfma_f32_32x32x16_bf16 v[60:75], v[40:43], v[32:35], 0\n\t" \
    "v_exp_f32 v84, v84\n\t" \
    "v_exp_f32 v85, v85\n\t" \
    "v_exp_f32 v86, v86\n\t" \
    "v_exp_f32 v87, v87\n\t" \
    "v_exp_f32 v88, v88\n\t" \
    "v_exp_f32 v89, v89\n\t" \
    "v_exp_f32 v90, v90\n\t" \
    "v_exp_f32 v91, v91\n\t" \
    "v_cvt_pk_bf16_f32 v56, v84, v85\n\t" \
    "v_cvt_pk_bf16_f32 v57, v86, v87\n\t" \
    "v_cvt_pk_bf16_f32 v58, v88, v89\n\t" \
    "v_cvt_pk_bf16_f32 v59, v90, v91\n\t" \
    "s_waitcnt lgkmcnt(0)\n\t" \
    "v_smfmac_f32_32x32x32_bf16 v[108:123], v[44:47], v[52:59], v126\n\t" \
    "v_smfmac_f32_16x16x64_bf16 v[142:145], v[48:51], v[52:59], v137\n\t" \
    "s_nop 15\n\t" \
    "s_nop 3\n\t"

#define ATTN_ASM_REST \
    "s_mov_b32 %[all], 0\n\t" \
    "v_mov_b32 v44, 0\n\t" \
    "v_mov_b32 v45, 0\n\t" \
    "v_mov_b32 v46, 0\n\t" \
    "v_mov_b32 v47, 0\n\t" \
    "v_mov_b32 v52, 0\n\t" \
    "v_mov_b32 v53, 0\n\t" \
    "v_mov_b32 v54, 0\n\t" \
    "v_mov_b32 v55, 0\n\t" \
    "v_mov_b32 v56, 0\n\t" \
    "v_mov_b32 v57, 0\n\t" \
    "v_mov_b32 v58, 0\n\t" \
    "v_mov_b32 v59, 0\n\t" \
    "s_cmp_eq_u32 %[all], 1\n\t" \
    "s_cbranch_scc1 OK0_%=\n\t" \
    "ds_read_b128 v[128:131], v136\n\t" \
    "ds_read_b128 v[132:135], v136 offset:16\n\t" \
    "v_add_u32 v127, 4, v127\n\t" \
    "s_mov_b32 %[spin], 0\n\t" \
    "s_waitcnt lgkmcnt(0)\n\t" \
    "v_min3_u32 v128, v129, v130, v131\n\t" \
    "v_max3_u32 v129, v129, v130, v131\n\t" \
    "v_min3_u32 v128, v128, v132, v133\n\t" \
    "v_max3_u32 v129, v129, v132, v133\n\t" \
    "v_min3_u32 v128, v128, v134, v135\n\t" \
    "v_max3_u32 v129, v129, v134, v135\n\t" \
    "s_nop 0\n\t" \
    "v_readfirstlane_b32 %[tmp], v128\n\t" \
    "v_readfirstlane_b32 %[spin], v129\n\t" \
    "s_nop 1\n\t" \
    "s_cmp_eq_u32 %[tmp], 0\n\t" \
    "s_cbranch_scc1 ONE0_%=\n\t" \
    "s_cmp_gt_u32 %[spin], %[thr]\n\t" \
    "s_cbranch_scc1 BAIL_%=\n\t" \
    "s_mov_b32 %[all], 1\n\t" \
    "s_branch OK0_%=\n\t" \
    "ONE0_%=:\n\t" \
    "s_mov_b32 %[spin], 0\n\t" \
    "SPIN0_%=:\n\t" \
    "ds_read_b32 v128, v127\n\t" \
    "s_waitcnt lgkmcnt(0)\n\t" \
    "s_nop 0\n\t" \
    "v_readfirstlane_b32 %[tmp], v128\n\t" \
    "s_nop 1\n\t" \
    "s_cmp_lg_u32 %[tmp], 0\n\t" \
    "s_cbranch_scc1 RDY0_%=\n\t" \
    "s_sleep 1\n\t" \
    "s_add_u32 %[spin], %[spin], 1\n\t" \
    "s_cmp_lt_u32 %[spin], 0x40000\n\t" \
    "s_cbranch_scc1 SPIN0_%=\n\t" \
    "RDY0_%=:\n\t" \
    "s_cmp_gt_u32 %[tmp], %[thr]\n\t" \
    "s_cbranch_scc1 BAIL_%=\n\t" \
    "OK0_%=:\n\t" \
    "ds_read_b128 v[40:43], v124 offset:0\n\t" \
    "s_waitcnt lgkmcnt(0)\n\t" \
    "v_mfma_f32_32x32x16_bf16 v[60:75], v[40:43], v[32:35], 0\n\t" \
    "s_mov_b32 %[j], 1\n\t" \
    "s_nop 7\n\t" \
    "s_nop 3\n\t" \
    "LOOP_%=:\n\t" \
    "v_exp_f32 v60, v60\n\t" \
    "v_exp_f32 v61, v61\n\t" \
    "v_smfmac_f32_32x32x32_bf16 v[108:123], v[44:47], v[52:59], v126\n\t" \
    "ds_read_b128 v[44:47], v125 offset:0\n\t" \
    "v_exp_f32 v62, v62\n\t" \
    "v_exp_f32 v63, v63\n\t" \
    "v_exp_f32 v64, v64\n\t" \
    "v_exp_f32 v65, v65\n\t" \
    "v_smfmac_f32_16x16x64_bf16 v[142:145], v[48:51], v[52:59], v137\n\t" \
    "v_exp_f32 v66, v66\n\t" \
    "v_exp_f32 v67, v67\n\t" \
    "v_cvt_pk_bf16_f32 v52, v60, v61\n\t" \
    "v_cvt_pk_bf16_f32 v53, v62, v63\n\t" \
    "v_cvt_pk_bf16_f32 v54, v64, v65\n\t" \
    "v_cvt_pk_bf16_f32 v55, v66, v67\n\t" \
    "v_mfma_f32_32x32x16_bf16 v[76:91], v[40:43], v[36:39], 0\n\t" \
    "ds_read_b128 v[40:43], v124 offset:1024\n\t" \
    "v_exp_f32 v68, v68\n\t" \
    "v_exp_f32 v69, v69\n\t" \
    "v_exp_f32 v70, v70\n\t" \
    "v_exp_f32 v71, v71\n\t" \
    "v_exp_f32 v72, v72\n\t" \
    "v_exp_f32 v73, v73\n\t" \
    "v_exp_f32 v74, v74\n\t" \
    "v_exp_f32 v75, v75\n\t" \
    "v_cvt_pk_bf16_f32 v56, v68, v69\n\t" \
    "v_cvt_pk_bf16_f32 v57, v70, v71\n\t" \
    "v_cvt_pk_bf16_f32 v58, v72, v73\n\t" \
    "v_cvt_pk_bf16_f32 v59, v74, v75\n\t" \
    "v_exp_f32 v76, v76\n\t" \
    "v_exp_f32 v77, v77\n\t" \
    "s_waitcnt lgkmcnt(0)\n\t" \
    "v_smfmac_f32_32x32x32_bf16 v[92:107], v[44:47], v[52:59], v126\n\t" \
    "v_exp_f32 v78, v78\n\t" \
    "v_exp_f32 v79, v79\n\t" \
    "v_exp_f32 v80, v80\n\t" \
    "v_exp_f32 v81, v81\n\t" \
    "v_smfmac_f32_16x16x64_bf16 v[138:141], v[48:51], v[52:59], v137\n\t" \
    "v_exp_f32 v82, v82\n\t" \
    "v_exp_f32 v83, v83\n\t" \
    "v_cvt_pk_bf16_f32 v52, v76, v77\n\t" \
    "v_cvt_pk_bf16_f32 v53, v78, v79\n\t" \
    "v_cvt_pk_bf16_f32 v54, v80, v81\n\t" \
    "v_cvt_pk_bf16_f32 v55, v82, v83\n\t" \
    "v_mfma_f32_32x32x16_bf16 v[60:75], v[40:43], v[32:35], 0\n\t" \
    "v_exp_f32 v84, v84\n\t" \
    "v_exp_f32 v85, v85\n\t" \
    "v_exp_f32 v86, v86\n\t" \
    "v_exp_f32 v87, v87\n\t" \
    "v_exp_f32 v88, v88\n\t" \
    "v_exp_f32 v89, v89\n\t" \
    "v_exp_f32 v90, v90\n\t" \
    "v_exp_f32 v91, v91\n\t" \
    "v_cvt_pk_bf16_f32 v56, v84, v85\n\t" \
    "v_cvt_pk_bf16_f32 v57, v86, v87\n\t" \
    "v_cvt_pk_bf16_f32 v58, v88, v89\n\t" \
    "v_cvt_pk_bf16_f32 v59, v90, v91\n\t" \
    "v_exp_f32 v60, v60\n\t" \
    "v_exp_f32 v61, v61\n\t" \
    "v_smfmac_f32_32x32x32_bf16 v[108:123], v[44:47], v[52:59], v126\n\t" \
    "ds_read_b128 v[44:47], v125 offset:1024\n\t" \
    "v_exp_f32 v62, v62\n\t" \
    "v_exp_f32 v63, v63\n\t" \
    "v_exp_f32 v64, v64\n\t" \
    "v_exp_f32 v65, v65\n\t" \
    "v_smfmac_f32_16x16x64_bf16 v[142:145], v[48:51], v[52:59], v137\n\t" \
    "v_exp_f32 v66, v66\n\t" \
    "v_exp_f32 v67, v67\n\t" \
    "v_cvt_pk_bf16_f32 v52, v60, v61\n\t" \
    "v_cvt_pk_bf16_f32 v53, v62, v63\n\t" \
    "v_cvt_pk_bf16_f32 v54, v64, v65\n\t" \
    "v_cvt_pk_bf16_f32 v55, v66, v67\n\t" \
    "v_mfma_f32_32x32x16_bf16 v[76:91], v[40:43], v[36:39], 0\n\t" \
    "ds_read_b128 v[40:43], v124 offset:2048\n\t" \
    "v_exp_f32 v68, v68\n\t" \
    "v_exp_f32 v69, v69\n\t" \
    "v_exp_f32 v70, v70\n\t" \
    "v_exp_f32 v71, v71\n\t" \
    "v_exp_f32 v72, v72\n\t" \
    "v_exp_f32 v73, v73\n\t" \
    "v_exp_f32 v74, v74\n\t" \
    "v_exp_f32 v75, v75\n\t" \
    "v_cvt_pk_bf16_f32 v56, v68, v69\n\t" \
    "v_cvt_pk_bf16_f32 v57, v70, v71\n\t" \
    "v_cvt_pk_bf16_f32 v58, v72, v73\n\t" \
    "v_cvt_pk_bf16_f32 v59, v74, v75\n\t" \
    "v_exp_f32 v76, v76\n\t" \
    "v_exp_f32 v77, v77\n\t" \
    "s_waitcnt lgkmcnt(0)\n\t" \
    "v_smfmac_f32_32x32x32_bf16 v[92:107], v[44:47], v[52:59], v126\n\t" \
    "v_exp_f32 v78, v78\n\t" \
    "v_exp_f32 v79, v79\n\t" \
    "v_exp_f32 v80, v80\n\t" \
    "v_exp_f32 v81, v81\n\t" \
    "v_smfmac_f32_16x16x64_bf16 v[138:141], v[48:51], v[52:59], v137\n\t" \
    "v_exp_f32 v82, v82\n\t" \
    "v_exp_f32 v83, v83\n\t" \
    "v_cvt_pk_bf16_f32 v52, v76, v77\n\t" \
    "v_cvt_pk_bf16_f32 v53, v78, v79\n\t" \
    "v_cvt_pk_bf16_f32 v54, v80, v81\n\t" \
    "v_cvt_pk_bf16_f32 v55, v82, v83\n\t" \
    "v_mfma_f32_32x32x16_bf16 v[60:75], v[40:43], v[32:35], 0\n\t" \
    "v_exp_f32 v84, v84\n\t" \
    "v_exp_f32 v85, v85\n\t" \
    "v_exp_f32 v86, v86\n\t" \
    "v_exp_f32 v87, v87\n\t" \
    "v_exp_f32 v88, v88\n\t" \
    "v_exp_f32 v89, v89\n\t" \
    "v_exp_f32 v90, v90\n\t" \
    "v_exp_f32 v91, v91\n\t" \
    "v_cvt_pk_bf16_f32 v56, v84, v85\n\t" \
    "v_cvt_pk_bf16_f32 v57, v86, v87\n\t" \
    "v_cvt_pk_bf16_f32 v58, v88, v89\n\t" \
    "v_cvt_pk_bf16_f32 v59, v90, v91\n\t" \
    "v_exp_f32 v60, v60\n\t" \
    "v_exp_f32 v61, v61\n\t" \
    "v_smfmac_f32_32x32x32_bf16 v[108:123], v[44:47], v[52:59], v126\n\t" \
    "ds_read_b128 v[44:47], v125 offset:2048\n\t" \
    "v_exp_f32 v62, v62\n\t" \
    "v_exp_f32 v63, v63\n\t" \
    "v_exp_f32 v64, v64\n\t" \
    "v_exp_f32 v65, v65\n\t" \
    "v_smfmac_f32_16x16x64_bf16 v[142:145], v[48:51], v[52:59], v137\n\t" \
    "v_exp_f32 v66, v66\n\t" \
    "v_exp_f32 v67, v67\n\t" \
    "v_cvt_pk_bf16_f32 v52, v60, v61\n\t" \
    "v_cvt_pk_bf16_f32 v53, v62, v63\n\t" \
    "v_cvt_pk_bf16_f32 v54, v64, v65\n\t" \
    "v_cvt_pk_bf16_f32 v55, v66, v67\n\t" \
    "v_mfma_f32_32x32x16_bf16 v[76:91], v[40:43], v[36:39], 0\n\t" \
    "ds_read_b128 v[40:43], v124 offset:3072\n\t" \
    "v_exp_f32 v68, v68\n\t" \
    "v_exp_f32 v69, v69\n\t" \
    "v_exp_f32 v70, v70\n\t" \
    "v_exp_f32 v71, v71\n\t" \
    "v_exp_f32 v72, v72\n\t" \
    "v_exp_f32 v73, v73\n\t" \
    "v_exp_f32 v74, v74\n\t" \
    "v_exp_f32 v75, v75\n\t" \
    "v_cvt_pk_bf16_f32 v56, v68, v69\n\t" \
    "v_cvt_pk_bf16_f32 v57, v70, v71\n\t" \
    "v_cvt_pk_bf16_f32 v58, v72, v73\n\t" \
    "v_cvt_pk_bf16_f32 v59, v74, v75\n\t" \
    "v_exp_f32 v76, v76\n\t" \
    "v_exp_f32 v77, v77\n\t" \
    "s_waitcnt lgkmcnt(0)\n\t" \
    "v_smfmac_f32_32x32x32_bf16 v[92:107], v[44:47], v[52:59], v126\n\t" \
    "v_exp_f32 v78, v78\n\t" \
    "v_exp_f32 v79, v79\n\t" \
    "v_exp_f32 v80, v80\n\t" \
    "v_exp_f32 v81, v81\n\t" \
    "v_smfmac_f32_16x16x64_bf16 v[138:141], v[48:51], v[52:59], v137\n\t" \
    "v_exp_f32 v82, v82\n\t" \
    "v_exp_f32 v83, v83\n\t" \
    "v_cvt_pk_bf16_f32 v52, v76, v77\n\t" \
    "v_cvt_pk_bf16_f32 v53, v78, v79\n\t" \
    "v_cvt_pk_bf16_f32 v54, v80, v81\n\t" \
    "v_cvt_pk_bf16_f32 v55, v82, v83\n\t" \
    "v_mfma_f32_32x32x16_bf16 v[60:75], v[40:43], v[32:35], 0\n\t" \
    "v_exp_f32 v84, v84\n\t" \
    "v_exp_f32 v85, v85\n\t" \
    "v_exp_f32 v86, v86\n\t" \
    "v_exp_f32 v87, v87\n\t" \
    "v_exp_f32 v88, v88\n\t" \
    "v_exp_f32 v89, v89\n\t" \
    "v_exp_f32 v90, v90\n\t" \
    "v_exp_f32 v91, v91\n\t" \
    "v_cvt_pk_bf16_f32 v56, v84, v85\n\t" \
    "v_cvt_pk_bf16_f32 v57, v86, v87\n\t" \
    "v_cvt_pk_bf16_f32 v58, v88, v89\n\t" \
    "v_cvt_pk_bf16_f32 v59, v90, v91\n\t" \
    "s_add_u32 %[pc], %[pc], 1\n\t" \
    "s_and_b32 %[pc], %[pc], 7\n\t" \
    "s_cmp_eq_u32 %[pc], 0\n\t" \
    "s_cselect_b32 %[dk], 0x10000, 0\n\t" \
    "s_cselect_b32 %[dv], 0x10000, 0\n\t" \
    "s_sub_u32 %[dk], 0x2000, %[dk]\n\t" \
    "s_sub_u32 %[dv], 0x2000, %[dv]\n\t" \
    "s_cmp_eq_u32 %[j], 7\n\t" \
    "s_cbranch_scc0 CHK_%=\n\t" \
    "s_mov_b32 %[dk], 0\n\t" \
    "s_mov_b32 %[dv], 0\n\t" \
    "s_branch GO_%=\n\t" \
    "CHK_%=:\n\t" \
    "s_cmp_eq_u32 %[all], 1\n\t" \
    "s_cbranch_scc1 OK1_%=\n\t" \
    "ds_read_b128 v[128:131], v136\n\t" \
    "ds_read_b128 v[132:135], v136 offset:16\n\t" \
    "v_add_u32 v127, 4, v127\n\t" \
    "s_mov_b32 %[spin], 0\n\t" \
    "s_waitcnt lgkmcnt(0)\n\t" \
    "v_min3_u32 v128, v129, v130, v131\n\t" \
    "v_max3_u32 v129, v129, v130, v131\n\t" \
    "v_min3_u32 v128, v128, v132, v133\n\t" \
    "v_max3_u32 v129, v129, v132, v133\n\t" \
    "v_min3_u32 v128, v128, v134, v135\n\t" \
    "v_max3_u32 v129, v129, v134, v135\n\t" \
    "s_nop 0\n\t" \
    "v_readfirstlane_b32 %[tmp], v128\n\t" \
    "v_readfirstlane_b32 %[spin], v129\n\t" \
    "s_nop 1\n\t" \
    "s_cmp_eq_u32 %[tmp], 0\n\t" \
    "s_cbranch_scc1 ONE1_%=\n\t" \
    "s_cmp_gt_u32 %[spin], %[thr]\n\t" \
    "s_cbranch_scc1 BAIL_%=\n\t" \
    "s_mov_b32 %[all], 1\n\t" \
    "s_branch OK1_%=\n\t" \
    "ONE1_%=:\n\t" \
    "s_mov_b32 %[spin], 0\n\t" \
    "SPIN1_%=:\n\t" \
    "ds_read_b32 v128, v127\n\t" \
    "s_waitcnt lgkmcnt(0)\n\t" \
    "s_nop 0\n\t" \
    "v_readfirstlane_b32 %[tmp], v128\n\t" \
    "s_nop 1\n\t" \
    "s_cmp_lg_u32 %[tmp], 0\n\t" \
    "s_cbranch_scc1 RDY1_%=\n\t" \
    "s_sleep 1\n\t" \
    "s_add_u32 %[spin], %[spin], 1\n\t" \
    "s_cmp_lt_u32 %[spin], 0x40000\n\t" \
    "s_cbranch_scc1 SPIN1_%=\n\t" \
    "RDY1_%=:\n\t" \
    "s_cmp_gt_u32 %[tmp], %[thr]\n\t" \
    "s_cbranch_scc1 BAIL_%=\n\t" \
    "OK1_%=:\n\t" \
    "GO_%=:\n\t" \
    "v_exp_f32 v60, v60\n\t" \
    "v_exp_f32 v61, v61\n\t" \
    "v_smfmac_f32_32x32x32_bf16 v[108:123], v[44:47], v[52:59], v126\n\t" \
    "ds_read_b128 v[44:47], v125 offset:3072\n\t" \
    "v_exp_f32 v62, v62\n\t" \
    "v_exp_f32 v63, v63\n\t" \
    "v_exp_f32 v64, v64\n\t" \
    "v_exp_f32 v65, v65\n\t" \
    "v_smfmac_f32_16x16x64_bf16 v[142:145], v[48:51], v[52:59], v137\n\t" \
    "v_exp_f32 v66, v66\n\t" \
    "v_exp_f32 v67, v67\n\t" \
    "v_cvt_pk_bf16_f32 v52, v60, v61\n\t" \
    "v_cvt_pk_bf16_f32 v53, v62, v63\n\t" \
    "v_cvt_pk_bf16_f32 v54, v64, v65\n\t" \
    "v_cvt_pk_bf16_f32 v55, v66, v67\n\t" \
    "v_mfma_f32_32x32x16_bf16 v[76:91], v[40:43], v[36:39], 0\n\t" \
    "v_add_u32 v124, %[dk], v124\n\t" \
    "ds_read_b128 v[40:43], v124 offset:0\n\t" \
    "v_exp_f32 v68, v68\n\t" \
    "v_exp_f32 v69, v69\n\t" \
    "v_exp_f32 v70, v70\n\t" \
    "v_exp_f32 v71, v71\n\t" \
    "v_exp_f32 v72, v72\n\t" \
    "v_exp_f32 v73, v73\n\t" \
    "v_exp_f32 v74, v74\n\t" \
    "v_exp_f32 v75, v75\n\t" \
    "v_cvt_pk_bf16_f32 v56, v68, v69\n\t" \
    "v_cvt_pk_bf16_f32 v57, v70, v71\n\t" \
    "v_cvt_pk_bf16_f32 v58, v72, v73\n\t" \
    "v_cvt_pk_bf16_f32 v59, v74, v75\n\t" \
    "v_exp_f32 v76, v76\n\t" \
    "v_exp_f32 v77, v77\n\t" \
    "s_waitcnt lgkmcnt(0)\n\t" \
    "v_smfmac_f32_32x32x32_bf16 v[92:107], v[44:47], v[52:59], v126\n\t" \
    "v_exp_f32 v78, v78\n\t" \
    "v_exp_f32 v79, v79\n\t" \
    "v_exp_f32 v80, v80\n\t" \
    "v_exp_f32 v81, v81\n\t" \
    "v_smfmac_f32_16x16x64_bf16 v[138:141], v[48:51], v[52:59], v137\n\t" \
    "v_exp_f32 v82, v82\n\t" \
    "v_exp_f32 v83, v83\n\t" \
    "v_cvt_pk_bf16_f32 v52, v76, v77\n\t" \
    "v_cvt_pk_bf16_f32 v53, v78, v79\n\t" \
    "v_cvt_pk_bf16_f32 v54, v80, v81\n\t" \
    "v_cvt_pk_bf16_f32 v55, v82, v83\n\t" \
    "v_mfma_f32_32x32x16_bf16 v[60:75], v[40:43], v[32:35], 0\n\t" \
    "v_add_u32 v125, %[dv], v125\n\t" \
    "v_exp_f32 v84, v84\n\t" \
    "v_exp_f32 v85, v85\n\t" \
    "v_exp_f32 v86, v86\n\t" \
    "v_exp_f32 v87, v87\n\t" \
    "v_exp_f32 v88, v88\n\t" \
    "v_exp_f32 v89, v89\n\t" \
    "v_exp_f32 v90, v90\n\t" \
    "v_exp_f32 v91, v91\n\t" \
    "v_cvt_pk_bf16_f32 v56, v84, v85\n\t" \
    "v_cvt_pk_bf16_f32 v57, v86, v87\n\t" \
    "v_cvt_pk_bf16_f32 v58, v88, v89\n\t" \
    "v_cvt_pk_bf16_f32 v59, v90, v91\n\t" \
    "s_add_u32 %[j], %[j], 1\n\t" \
    "s_cmp_lg_u32 %[j], 4\n\t" \
    "s_cbranch_scc1 NOSW_%=\n\t" \
    "s_cmp_eq_u32 %[half], 0\n\t" \
    "s_cbranch_scc1 SWA_%=\n\t" \
    "s_setprio 0\n\t" \
    "s_branch NOSW_%=\n\t" \
    "SWA_%=:\n\t" \
    "s_setprio 1\n\t" \
    "NOSW_%=:\n\t" \
    "s_cmp_lt_u32 %[j], 8\n\t" \
    "s_cbranch_scc1 LOOP_%=\n\t" \
    "s_waitcnt lgkmcnt(0)\n\t" \
    "v_smfmac_f32_32x32x32_bf16 v[108:123], v[44:47], v[52:59], v126\n\t" \
    "v_smfmac_f32_16x16x64_bf16 v[142:145], v[48:51], v[52:59], v137\n\t" \
    "s_mov_b32 %[st], 0\n\t" \
    "s_branch END_%=\n\t" \
    "BAIL_%=:\n\t" \
    "s_waitcnt lgkmcnt(0)\n\t" \
    "s_mov_b32 %[st], 1\n\t" \
    "END_%=:\n\t" \
    "s_nop 15\n\t" \
    "s_nop 3\n\t"

#define CONV_ASM \
    "v_mov_b32 v40, 0\n\t" \
    "v_mov_b32 v41, 0\n\t" \
    "v_mov_b32 v42, 0\n\t" \
    "v_mov_b32 v43, 0\n\t" \
    "v_mov_b32 v44, 0\n\t" \
    "v_mov_b32 v45, 0\n\t" \
    "v_mov_b32 v46, 0\n\t" \
    "v_mov_b32 v47, 0\n\t" \
    "v_mov_b32 v48, 0\n\t" \
    "v_mov_b32 v49, 0\n\t" \
    "v_mov_b32 v50, 0\n\t" \
    "v_mov_b32 v51, 0\n\t" \
    "v_mov_b32 v52, 0\n\t" \
    "v_mov_b32 v53, 0\n\t" \
    "v_mov_b32 v54, 0\n\t" \
    "v_mov_b32 v55, 0\n\t" \
    "ds_read2_b32 v[56:57], v88 offset0:8 offset1:9\n\t" \
    "ds_read2_b32 v[58:59], v88 offset0:10 offset1:11\n\t" \
    "ds_read_b128 v[72:75], v89 offset:0\n\t" \
    "ds_read2_b32 v[60:61], v88 offset0:0 offset1:1\n\t" \
    "ds_read2_b32 v[62:63], v88 offset0:2 offset1:3\n\t" \
    "ds_read_b128 v[76:79], v89 offset:32\n\t" \
    "ds_read2_b32 v[64:65], v88 offset0:24 offset1:25\n\t" \
    "ds_read2_b32 v[66:67], v88 offset0:26 offset1:27\n\t" \
    "ds_read_b128 v[80:83], v90 offset:0\n\t" \
    "ds_read2_b32 v[68:69], v88 offset0:16 offset1:17\n\t" \
    "ds_read2_b32 v[70:71], v88 offset0:18 offset1:19\n\t" \
    "ds_read_b128 v[84:87], v90 offset:32\n\t" \
    "CLOOP_%=:\n\t" \
    "s_waitcnt lgkmcnt(9)\n\t" \
    "v_mfma_f32_32x32x16_bf16 v[40:55], v[56:59], v[72:75], v[40:55]\n\t" \
    "v_add_u32 v88, 128, v88\n\t" \
    "v_add_u32 v89, v91, v89\n\t" \
    "ds_read2_b32 v[56:57], v88 offset0:8 offset1:9\n\t" \
    "ds_read2_b32 v[58:59], v88 offset0:10 offset1:11\n\t" \
    "ds_read_b128 v[72:75], v89 offset:0\n\t" \
    "s_waitcnt lgkmcnt(9)\n\t" \
    "v_mfma_f32_32x32x16_bf16 v[40:55], v[60:63], v[76:79], v[40:55]\n\t" \
    "ds_read2_b32 v[60:61], v88 offset0:0 offset1:1\n\t" \
    "ds_read2_b32 v[62:63], v88 offset0:2 offset1:3\n\t" \
    "ds_read_b128 v[76:79], v89 offset:32\n\t" \
    "s_sleep 1\n\t" \
    "s_waitcnt lgkmcnt(9)\n\t" \
    "v_mfma_f32_32x32x16_bf16 v[40:55], v[64:67], v[80:83], v[40:55]\n\t" \
    "v_add_u32 v90, v91, v90\n\t" \
    "ds_read2_b32 v[64:65], v88 offset0:24 offset1:25\n\t" \
    "ds_read2_b32 v[66:67], v88 offset0:26 offset1:27\n\t" \
    "ds_read_b128 v[80:83], v90 offset:0\n\t" \
    "s_waitcnt lgkmcnt(9)\n\t" \
    "v_mfma_f32_32x32x16_bf16 v[40:55], v[68:71], v[84:87], v[40:55]\n\t" \
    "ds_read2_b32 v[68:69], v88 offset0:16 offset1:17\n\t" \
    "ds_read2_b32 v[70:71], v88 offset0:18 offset1:19\n\t" \
    "ds_read_b128 v[84:87], v90 offset:32\n\t" \
    "s_sleep 1\n\t" \
    "s_sub_u32 %[m], %[m], 1\n\t" \
    "s_cmp_lg_u32 %[m], 0\n\t" \
    "s_cbranch_scc1 CLOOP_%=\n\t" \
    "s_waitcnt lgkmcnt(0)\n\t" \
    "s_nop 15\n\t"

#define PROJ_ASM \
    "ds_read_b128 v[20:23], v16 offset:0\n\t" \
    "ds_read_b128 v[24:27], v16 offset:1024\n\t" \
    "ds_read_b128 v[28:31], v16 offset:2048\n\t" \
    "s_waitcnt lgkmcnt(2)\n\t" \
    "v_mfma_f32_32x32x16_bf16 v[0:15], v[32:35], v[20:23], 0\n\t" \
    "ds_read_b128 v[20:23], v16 offset:3072\n\t" \
    "ds_read_b128 v[32:35], v16 offset:4096\n\t" \
    "s_waitcnt lgkmcnt(3)\n\t" \
    "v_mfma_f32_32x32x16_bf16 v[0:15], v[36:39], v[24:27], v[0:15]\n\t" \
    "ds_read_b128 v[24:27], v16 offset:5120\n\t" \
    "ds_read_b128 v[36:39], v16 offset:6144\n\t" \
    "s_waitcnt lgkmcnt(4)\n\t" \
    "v_mfma_f32_32x32x16_bf16 v[0:15], v[40:43], v[28:31], v[0:15]\n\t" \
    "ds_read_b128 v[28:31], v16 offset:7168\n\t" \
    "ds_read_b128 v[40:43], v16 offset:8192\n\t" \
    "s_waitcnt lgkmcnt(5)\n\t" \
    "v_mfma_f32_32x32x16_bf16 v[0:15], v[44:47], v[20:23], v[0:15]\n\t" \
    "ds_read_b128 v[20:23], v16 offset:9216\n\t" \
    "ds_read_b128 v[44:47], v16 offset:10240\n\t" \
    "s_waitcnt lgkmcnt(6)\n\t" \
    "v_mfma_f32_32x32x16_bf16 v[0:15], v[48:51], v[32:35], v[0:15]\n\t" \
    "ds_read_b128 v[32:35], v16 offset:11264\n\t" \
    "s_waitcnt lgkmcnt(6)\n\t" \
    "v_mfma_f32_32x32x16_bf16 v[0:15], v[52:55], v[24:27], v[0:15]\n\t" \
    "ds_read_b128 v[48:51], v16 offset:12288\n\t" \
    "s_waitcnt lgkmcnt(6)\n\t" \
    "v_mfma_f32_32x32x16_bf16 v[0:15], v[56:59], v[36:39], v[0:15]\n\t" \
    "ds_read_b128 v[24:27], v16 offset:13312\n\t" \
    "s_waitcnt lgkmcnt(6)\n\t" \
    "v_mfma_f32_32x32x16_bf16 v[0:15], v[60:63], v[28:31], v[0:15]\n\t" \
    "ds_read_b128 v[52:55], v16 offset:14336\n\t" \
    "s_waitcnt lgkmcnt(6)\n\t" \
    "v_mfma_f32_32x32x16_bf16 v[0:15], v[64:67], v[40:43], v[0:15]\n\t" \
    "ds_read_b128 v[36:39], v16 offset:15360\n\t" \
    "s_waitcnt lgkmcnt(6)\n\t" \
    "v_mfma_f32_32x32x16_bf16 v[0:15], v[68:71], v[20:23], v[0:15]\n\t" \
    "ds_read_b128 v[56:59], v16 offset:16384\n\t" \
    "s_waitcnt lgkmcnt(6)\n\t" \
    "v_mfma_f32_32x32x16_bf16 v[0:15], v[72:75], v[44:47], v[0:15]\n\t" \
    "ds_read_b128 v[28:31], v16 offset:17408\n\t" \
    "s_waitcnt lgkmcnt(6)\n\t" \
    "v_mfma_f32_32x32x16_bf16 v[0:15], v[76:79], v[32:35], v[0:15]\n\t" \
    "ds_read_b128 v[60:63], v16 offset:18432\n\t" \
    "s_waitcnt lgkmcnt(6)\n\t" \
    "v_mfma_f32_32x32x16_bf16 v[0:15], v[80:83], v[48:51], v[0:15]\n\t" \
    "ds_read_b128 v[40:43], v16 offset:19456\n\t" \
    "s_waitcnt lgkmcnt(6)\n\t" \
    "v_mfma_f32_32x32x16_bf16 v[0:15], v[84:87], v[24:27], v[0:15]\n\t" \
    "ds_read_b128 v[64:67], v16 offset:20480\n\t" \
    "s_waitcnt lgkmcnt(6)\n\t" \
    "v_mfma_f32_32x32x16_bf16 v[0:15], v[88:91], v[52:55], v[0:15]\n\t" \
    "ds_read_b128 v[20:23], v16 offset:21504\n\t" \
    "s_waitcnt lgkmcnt(6)\n\t" \
    "v_mfma_f32_32x32x16_bf16 v[0:15], v[92:95], v[36:39], v[0:15]\n\t" \
    "ds_read_b128 v[68:71], v16 offset:22528\n\t" \
    "s_waitcnt lgkmcnt(6)\n\t" \
    "v_mfma_f32_32x32x16_bf16 v[0:15], v[96:99], v[56:59], v[0:15]\n\t" \
    "ds_read_b128 v[44:47], v16 offset:23552\n\t" \
    "s_waitcnt lgkmcnt(6)\n\t" \
    "v_mfma_f32_32x32x16_bf16 v[0:15], v[100:103], v[28:31], v[0:15]\n\t" \
    "ds_read_b128 v[72:75], v16 offset:24576\n\t" \
    "s_waitcnt lgkmcnt(6)\n\t" \
    "v_mfma_f32_32x32x16_bf16 v[0:15], v[104:107], v[60:63], v[0:15]\n\t" \
    "ds_read_b128 v[32:35], v16 offset:25600\n\t" \
    "s_waitcnt lgkmcnt(6)\n\t" \
    "v_mfma_f32_32x32x16_bf16 v[0:15], v[108:111], v[40:43], v[0:15]\n\t" \
    "ds_read_b128 v[76:79], v16 offset:26624\n\t" \
    "s_waitcnt lgkmcnt(6)\n\t" \
    "v_mfma_f32_32x32x16_bf16 v[0:15], v[112:115], v[64:67], v[0:15]\n\t" \
    "ds_read_b128 v[48:51], v16 offset:27648\n\t" \
    "s_waitcnt lgkmcnt(6)\n\t" \
    "v_mfma_f32_32x32x16_bf16 v[0:15], v[116:119], v[20:23], v[0:15]\n\t" \
    "ds_read_b128 v[80:83], v16 offset:28672\n\t" \
    "s_waitcnt lgkmcnt(6)\n\t" \
    "v_mfma_f32_32x32x16_bf16 v[0:15], v[120:123], v[68:71], v[0:15]\n\t" \
    "ds_read_b128 v[24:27], v16 offset:29696\n\t" \
    "s_waitcnt lgkmcnt(6)\n\t" \
    "v_mfma_f32_32x32x16_bf16 v[0:15], v[124:127], v[44:47], v[0:15]\n\t" \
    "ds_read_b128 v[84:87], v16 offset:30720\n\t" \
    "s_waitcnt lgkmcnt(6)\n\t" \
    "v_mfma_f32_32x32x16_bf16 v[0:15], v[128:131], v[72:75], v[0:15]\n\t" \
    "ds_read_b128 v[52:55], v16 offset:31744\n\t" \
    "s_waitcnt lgkmcnt(6)\n\t" \
    "v_mfma_f32_32x32x16_bf16 v[0:15], v[132:135], v[32:35], v[0:15]\n\t" \
    "s_waitcnt lgkmcnt(5)\n\t" \
    "v_mfma_f32_32x32x16_bf16 v[0:15], v[136:139], v[76:79], v[0:15]\n\t" \
    "s_waitcnt lgkmcnt(4)\n\t" \
    "v_mfma_f32_32x32x16_bf16 v[0:15], v[140:143], v[48:51], v[0:15]\n\t" \
    "s_waitcnt lgkmcnt(3)\n\t" \
    "v_mfma_f32_32x32x16_bf16 v[0:15], v[144:147], v[80:83], v[0:15]\n\t" \
    "s_waitcnt lgkmcnt(2)\n\t" \
    "v_mfma_f32_32x32x16_bf16 v[0:15], v[148:151], v[24:27], v[0:15]\n\t" \
    "s_waitcnt lgkmcnt(1)\n\t" \
    "v_mfma_f32_32x32x16_bf16 v[0:15], v[152:155], v[84:87], v[0:15]\n\t" \
    "s_waitcnt lgkmcnt(0)\n\t" \
    "v_mfma_f32_32x32x16_bf16 v[0:15], v[156:159], v[52:55], v[0:15]\n\t" \
    "s_nop 15\n\t"

__global__ __launch_bounds__(768, 3) void kf_fused(const unsigned short* __restrict__ u, const unsigned short* __restrict__ logA,
                                                const unsigned short* __restrict__ aim, const unsigned short* __restrict__ cre,
                                                const unsigned short* __restrict__ cim, const unsigned short* __restrict__ logdt,
                                                const unsigned short* __restrict__ dskip, const unsigned short* __restrict__ x,
                                                const unsigned short* __restrict__ wqkv, const unsigned short* __restrict__ wo,
                                                char* __restrict__ ws, unsigned short* __restrict__ out) {
    __shared__ __attribute__((aligned(16))) char smem[LDS_BYTES];
    LDSP char* const sm = (LDSP char*)smem;
    const unsigned lds0 = (unsigned)(uintptr_t)sm;
    const int lane = threadIdx.x & 63, w = __builtin_amdgcn_readfirstlane(threadIdx.x >> 6), b = blockIdx.x;
    const int r = lane & 31, h = lane >> 5;
    const int rb = 4 * (b & 1) + (b >> 6), head = 8 * ((b >> 1) & 3) + ((b >> 3) & 7);
    const int wg = head * 8 + rb;
    asm volatile("" :: "s"(u), "s"(logA), "s"(aim), "s"(cre), "s"(cim), "s"(logdt), "s"(dskip), "s"(x), "s"(wqkv), "s"(wo), "s"(ws), "s"(out));
    u32x2 epv;
    asm volatile("global_load_dwordx2 %0, %1, %2 sc1" : "=&v"(epv) : "v"(0), "s"(ws + WS_EP) : "memory");
    auto get_tag = [&]() {
        asm volatile("" : "+v"(epv));
        return (((unsigned long long)(unsigned)__builtin_amdgcn_readfirstlane(epv[1]) << 32) | (unsigned)__builtin_amdgcn_readfirstlane(epv[0])) + 1;
    };
    if (w < 8) {
        {
            unsigned pf = 0;
            if (w < 2) {
                const int l6 = lane < 48 ? lane : 0, wr = l6 / 6, ws6 = l6 - 6 * wr;
                const unsigned wo_ = (unsigned)(16 * (b >> 3) + 8 * w + wr) * 3072 + (ws6 >> 1) * 1024 + ((b >> 1) & 3) * 256 + (ws6 & 1) * 128;
                asm volatile("global_load_dword %0, %1, %2" : "+v"(pf) : "v"(wo_), "s"(wqkv) : "memory");
            }
            const unsigned xa0 = (w * 32 + r) * 128, xsw = (r >> 1) & 7;
            const unsigned wa = ST_W + (8 * h + ((lane & 15) >> 2)) * 64 + (((lane >> 4) & 1) * 16 + (lane & 3) * 4) * 2;
            const unsigned wva = ST_WV + (8 * h + ((lane & 15) >> 2)) * 32 + (lane & 3) * 8;
            f32x16 aqk = {}, av = {};
            bf16x8 xf[4], wq[4], wv[4];
            auto m1 = [&](int t, int j) {
                if (j == 0) aqk = mfma_bf16(wq[t], xf[t], aqk);
                else av = mfma_bf16(xf[t], wv[t], av);
            };
#pragma unroll
            for (int s = 0; s < 8; ++s) {
                if (s == 0) BAR_LGKM();
                else asm volatile("s_waitcnt lgkmcnt(0)\n\ts_barrier" : "+v"(xf[2]), "+v"(wq[2]), "+v"(wv[2]), "+v"(xf[3]), "+v"(wq[3]), "+v"(wv[3]) :: "memory");
                unsigned so = (s & 3) * STAGE;
                asm volatile("" : "+v"(so));
                const LDSP char* wp = sm + (wa + so);
                const LDSP char* wvp = sm + (wva + so);
                auto rx = [&](int t) { xf[t] = *(const LDSP bf16x8*)(sm + (xa0 + (((2 * t + h) ^ xsw) << 4) + so)); wq[t] = tr_frag(wp, t * 1024, t * 1024 + 256); };
                auto rv = [&](int t) { wv[t] = tr_frag(wvp, t * 512, t * 512 + 128); };
#pragma unroll
                for (int g = 0; g < 8; ++g) {
                    if (s > 0 || g >= 4) m1((g / 2 + 2) & 3, g & 1);
                    __builtin_amdgcn_sched_barrier(0);
                    if (g & 1) rv(g >> 1); else rx(g >> 1);
                    __builtin_amdgcn_sched_barrier(0);
                }
            }
#pragma unroll
            for (int g = 0; g < 4; ++g) m1(2 + (g >> 1), g & 1);
            WAIT_VM(0);
            asm volatile("" : "+v"(pf));
            BAR_LGKM();
            const int T = rb * 8 + w;
            *(LDSP u32x4*)(sm + L_K + T * 1024 + lane * 16) = pack8(aqk, 8, 1.0f);
            if (r < 16) {
#pragma unroll
                for (int q = 0; q < 4; ++q) {
                    const int hh = q >> 1, par = q & 1, j = 8 * hh + 2 * par;
                    const u32x2 v = {cvtpk_bf16(av[j], av[j + 1]), cvtpk_bf16(av[j + 4], av[j + 5])};
                    *(LDSP u32x2*)(sm + L_V + T * VT + hh * 512 + (16 * par + r) * 16 + h * 8) = v;
                }
            }
            BAR_LGKM();
            *(LDSP u32x4*)(sm + L_QX + w * 1024 + lane * 16) = pack8(aqk, 0, QSCALE);
            float kn = 0.f, qn = 0.f, vmx = 0.f;
#pragma unroll
            for (int e = 0; e < 8; ++e) { qn = ffma1(aqk[e], aqk[e], qn); kn = ffma1(aqk[8 + e], aqk[8 + e], kn); vmx = max3abs(av[2 * e], av[2 * e + 1], vmx); }
            kn = half_sum(kn); qn = half_sum(qn) * (QSCALE * QSCALE);
            if (!(vmx <= 0x1p60f)) kn = INFINITY;
            wave_max2(kn, qn);
            if (lane == 0) { *(LDSP float*)(sm + L_QX + 8192 + w * 4) = kn; *(LDSP float*)(sm + L_QX + 8224 + w * 4) = qn; }
        }
        const unsigned long long tag = get_tag();
        BAR_LGKM();
        const int T = rb * 8 + w;
        const u32x4 qa = *(const LDSP u32x4*)(sm + L_QX + (w & ~1) * 1024 + lane * 16), qb = *(const LDSP u32x4*)(sm + L_QX + (w | 1) * 1024 + lane * 16);
        const u32x4 kma = *(const LDSP u32x4*)(sm + L_QX + 8192), kmb = *(const LDSP u32x4*)(sm + L_QX + 8208);
        const u32x2 qn2 = *(const LDSP u32x2*)(sm + L_QX + 8224 + (w & ~1) * 4);
        BAR_LGKM();
        const int kh = w & 1;
        const float km_own = max3f(max3f(__uint_as_float(kma[0]), __uint_as_float(kma[1]), __uint_as_float(kma[2])),
                                   max3f(__uint_as_float(kma[3]), __uint_as_float(kmb[0]), __uint_as_float(kmb[1])), fmaxf(__uint_as_float(kmb[2]), __uint_as_float(kmb[3])));
        const float kthr_f = fminf((SAFE_BOUND * SAFE_BOUND / 1.045f) * __builtin_amdgcn_rcpf(fmaxf(__uint_as_float(qn2[0]), __uint_as_float(qn2[1]))), 3.0e38f);
        const unsigned thr = __builtin_amdgcn_readfirstlane(__float_as_uint(kthr_f));
        const unsigned t0 = 8 * rb + 4 * kh;
        const unsigned ka0 = lds0 + L_K + t0 * 1024 + lane * 16;
        const unsigned va0 = lds0 + L_V + t0 * VT + lane * 16;
        const unsigned idxp = r < 16 ? 0x4444u : 0xeeeeu, idxl = (lane & 1) ? 0xeeeeu : 0x4444u;
        const unsigned one2 = 0x3f803f80u, slo = (lane & 15) < 2 ? one2 : 0u, shi = ((lane & 15) >> 1) == 1 ? one2 : 0u;
        u32x4 lsel = {slo, slo, shi, shi};
        f32x16 oa, ob;
        f32x4 la, lb;
        float ma = 0.f, mb = 0.f;
        unsigned st = !(km_own <= kthr_f);
#define ATTN_CLOBBERS "v40", "v41", "v42", "v43", "v44", "v45", "v46", "v47", "v52", "v53", "v54", "v55", "v56", "v57", \
                      "v58", "v59", "v60", "v61", "v62", "v63", "v64", "v65", "v66", "v67", "v68", "v69", "v70", "v71", "v72", "v73", "v74", "v75", \
                      "v76", "v77", "v78", "v79", "v80", "v81", "v82", "v83", "v84", "v85", "v86", "v87", "v88", "v89", "v90", "v91", \
                      "v128", "v129", "v130", "v131", "v132", "v133", "v134", "v135", "scc", "memory"
        u32x4 qa_ = qa, qb_ = qb;
        const int pc = (rb + w) & 7;
        const gu64* f1 = (const gu64*)(ws + WS_F1 + (8 * head + pc) * 128);
        float kmv = 0.f;
        auto fetch = [&]() {
            const char* kg = ws + WS_K + ((size_t)head * 64 + 8 * pc) * 1024;
            const char* vg = ws + WS_V + ((size_t)head * 64 + 8 * pc) * 1024;
#pragma unroll
            for (int i = 0; i < 8; ++i) {
                glds16_dev(kg + i * 1024, lane * 16, lds0 + L_K + (8 * pc + i) * 1024);
                glds16_dev(vg + i * 1024, lane * 16, lds0 + L_V + (8 * pc + i) * VT);
            }
            asm volatile("global_load_dword %0, %1, %2 sc1" : "+v"(kmv) : "v"((lane & 7) * 4), "s"(ws + WS_KM + (head * 64 + 8 * pc) * 4) : "memory");
        };
        bool fetched = false;
        if (!st) {
            unsigned vk = ka0, vv = va0, ip = idxp, il = idxl;
            u32x4 pk, pv0; u32x8 pp; f32x16 psa, psb;
            asm volatile(ATTN_ASM_OWN_A
                         : "+{v[32:35]}"(qa_), "+{v[36:39]}"(qb_), "={v[92:107]}"(oa), "={v[108:123]}"(ob), "+{v124}"(vk), "+{v125}"(vv), "+{v126}"(ip), "+{v137}"(il),
                           "={v[40:43]}"(pk), "={v[44:47]}"(pv0), "+{v[48:51]}"(lsel), "={v[52:59]}"(pp), "={v[60:75]}"(psa), "={v[76:91]}"(psb),
                           "={v[138:141]}"(la), "={v[142:145]}"(lb)
                         :: "scc", "memory");
            if (w == 1 && __all(__hip_atomic_load(f1, RLX_AGENT) == tag)) { asm volatile("" ::: "memory"); fetch(); fetched = true; }
            asm volatile(ATTN_ASM_OWN_B
                         : "+{v[32:35]}"(qa_), "+{v[36:39]}"(qb_), "+{v[92:107]}"(oa), "+{v[108:123]}"(ob), "+{v124}"(vk), "+{v125}"(vv), "+{v126}"(ip), "+{v137}"(il),
                           "+{v[40:43]}"(pk), "+{v[44:47]}"(pv0), "+{v[48:51]}"(lsel), "+{v[52:59]}"(pp), "+{v[60:75]}"(psa), "+{v[76:91]}"(psb),
                           "+{v[138:141]}"(la), "+{v[142:145]}"(lb)
                         :: "scc", "memory");
        }
        if (w > 0) {
            if (!fetched) {
                if (w >= 4) wait_lds_word((const LDSP volatile unsigned*)(sm + L_FLAG + 32 + 12), 4);
                wait_tags(f1, tag);
                fetch();
            }
            asm volatile("s_waitcnt vmcnt(0)" : "+v"(kmv) :: "memory");
            const unsigned kb = __float_as_uint(wave_max(kmv));
            if (lane == 0) *(LDSP volatile unsigned*)(sm + L_FLAG + 4 * w) = kb > 1u ? kb : 1u;
        }
        if (!st) {
            if (w >= 4) __builtin_amdgcn_s_setprio(1);
            const int d1 = (int)((rb + 1) & 7) - rb;
            unsigned vk = ka0 + d1 * 8192, vv = va0 + d1 * (8 * VT), vf = lds0 + L_FLAG, vb = lds0 + L_FLAG, ip = idxp, il = idxl;
            unsigned sj, spc = (rb + 1) & 7, sdk, sdv, sspin, stmp, sall;
            asm volatile(ATTN_ASM_REST
                         : "+{v[32:35]}"(qa_), "+{v[36:39]}"(qb_), "+{v[92:107]}"(oa), "+{v[108:123]}"(ob),
                           "+{v124}"(vk), "+{v125}"(vv), "+{v126}"(ip), "+{v127}"(vf), "+{v136}"(vb), "+{v137}"(il),
                           "+{v[48:51]}"(lsel), "+{v[138:141]}"(la), "+{v[142:145]}"(lb),
                           [j] "=&s"(sj), [pc] "+s"(spc), [dk] "=&s"(sdk), [dv] "=&s"(sdv), [spin] "=&s"(sspin), [tmp] "=&s"(stmp), [all] "=&s"(sall), [st] "=&s"(st)
                         : [thr] "s"(thr), [half] "s"(w >> 2)
                         : ATTN_CLOBBERS);
            __builtin_amdgcn_s_setprio(0);
        }
        if (st) {
#pragma unroll 1
            for (int j = 1; j < 8; ++j) wait_lds_word((const LDSP volatile unsigned*)(sm + L_FLAG + 4 * j), 1);
            const LDSP char* kp = sm + L_K + 4 * kh * 1024 + lane * 16;
            const LDSP char* vp = sm + L_V + 4 * kh * VT + lane * 16;
            const f32x16 zero = {};
            ma = -INFINITY; mb = -INFINITY;
#pragma unroll 1
            for (int i = 0; i < 32; ++i) {
                const int t = (i >> 2) * 8 + (i & 3);
                const bf16x8 kf = *(const LDSP bf16x8*)(kp + t * 1024);
                const f32x16 sa = mfma_bf16(kf, as_bf(qa), zero), sb = mfma_bf16(kf, as_bf(qb), zero);
#pragma unroll
                for (int e = 0; e < 16; ++e) { ma = fmaxf(ma, sa[e]); mb = fmaxf(mb, sb[e]); }
            }
            ma = half_max(ma); mb = half_max(mb);
            oa = zero; ob = zero; la = (f32x4){0.f, 0.f, 0.f, 0.f}; lb = la;
#pragma unroll 1
            for (int i = 0; i < 32; ++i) {
                const int t = (i >> 2) * 8 + (i & 3);
                const bf16x8 kf = *(const LDSP bf16x8*)(kp + t * 1024), vf = *(const LDSP bf16x8*)(vp + t * VT);
                const f32x16 sa = mfma_bf16(kf, as_bf(qa), zero), sb = mfma_bf16(kf, as_bf(qb), zero);
                u32x8 pp;
#pragma unroll
                for (int e = 0; e < 8; ++e) pp[e] = cvtpk_bf16(__builtin_amdgcn_exp2f(sa[2 * e] - ma), __builtin_amdgcn_exp2f(sa[2 * e + 1] - ma));
                oa = __builtin_amdgcn_smfmac_f32_32x32x32_bf16(vf, __builtin_bit_cast(bf16x16, pp), oa, (int)idxp, 0, 0);
                la = __builtin_amdgcn_smfmac_f32_16x16x64_bf16(as_bf(lsel), __builtin_bit_cast(bf16x16, pp), la, (int)idxl, 0, 0);
#pragma unroll
                for (int e = 0; e < 8; ++e) pp[e] = cvtpk_bf16(__builtin_amdgcn_exp2f(sb[2 * e] - mb), __builtin_amdgcn_exp2f(sb[2 * e + 1] - mb));
                ob = __builtin_amdgcn_smfmac_f32_32x32x32_bf16(vf, __builtin_bit_cast(bf16x16, pp), ob, (int)idxp, 0, 0);
                lb = __builtin_amdgcn_smfmac_f32_16x16x64_bf16(as_bf(lsel), __builtin_bit_cast(bf16x16, pp), lb, (int)idxl, 0, 0);
            }
        }
        float lsa, lsb;
        {
#pragma unroll
            for (int i = 0; i < 8; ++i) { oa[i] += oa[8 + i]; ob[i] += ob[8 + i]; }
            const int src = (lane & 15) * 4;
            const float a0 = __int_as_float(__builtin_amdgcn_ds_bpermute(src, __float_as_int(la[0] + la[1]))), a1 = __int_as_float(__builtin_amdgcn_ds_bpermute(src, __float_as_int(la[2] + la[3])));
            const float b0 = __int_as_float(__builtin_amdgcn_ds_bpermute(src, __float_as_int(lb[0] + lb[1]))), b1 = __int_as_float(__builtin_amdgcn_ds_bpermute(src, __float_as_int(lb[2] + lb[3])));
            lsa = r < 16 ? a0 : a1; lsb = r < 16 ? b0 : b1;
        }
        BAR_LGKM();
        {
            const f32x16& og = kh ? oa : ob;
            const float mg = kh ? ma : mb;
            LDSP char* pp = sm + L_PART + w * PART_SZ + lane * 16;
            *(LDSP f32x4*)(pp) = (f32x4){og[0], og[1], og[2], og[3]};
            *(LDSP f32x4*)(pp + 1024) = (f32x4){og[4], og[5], og[6], og[7]};
            *(LDSP f32x2*)(pp + 2048) = (f32x2){kh ? lsa : lsb, mg};
            if (w == 0 && lane == 0) *(LDSP unsigned*)(sm + L_OCNT) = 0;
        }
        BAR_LGKM();
        {
            const f32x16& ok = kh ? ob : oa;
            const float mk = kh ? mb : ma;
            const LDSP char* pp = sm + L_PART + (w ^ 1) * PART_SZ + lane * 16;
            const f32x4 p0 = *(const LDSP f32x4*)(pp), p1 = *(const LDSP f32x4*)(pp + 1024);
            const f32x2 p2 = *(const LDSP f32x2*)(pp + 2048);
            const float mo = p2[1], mm = fmaxf(mk, mo);
            const float fk = __builtin_amdgcn_exp2f(mk - mm), fo = __builtin_amdgcn_exp2f(mo - mm);
            const float lsum = (kh ? lsb : lsa) * fk + p2[0] * fo;
            const float rl = __builtin_amdgcn_rcpf(lsum) * (2.0f - lsum * __builtin_amdgcn_rcpf(lsum));
            f32x16 of;
#pragma unroll
            for (int i = 0; i < 4; ++i) { of[i] = ok[i] * fk + p0[i] * fo; of[4 + i] = ok[4 + i] * fk + p1[i] * fo; }
            store16_dev(ws + WS_O + ((size_t)T * 32 + head) * 1024, lane * 16, pack8(of, 0, rl));
        }
        WAIT_VM(0);
        if (lane == 0 && __hip_atomic_fetch_add((LDSP unsigned*)(sm + L_OCNT), 1u, __ATOMIC_RELAXED, __HIP_MEMORY_SCOPE_WORKGROUP) == 7)
            __hip_atomic_store((gu64*)(ws + WS_F2 + wg * 128), tag, RLX_AGENT);
        wait_tags((const gu64*)(ws + WS_F2 + (rb + 8 * (4 * w + (lane & 3))) * 128), tag);
        const int qt3 = rb * 8 + 2 * (head >> 3) + ((head >> 2) & 1);
        {
            const char* og = ws + WS_O + ((size_t)qt3 * 32 + 4 * w) * 1024;
            u32x4 o0, o1, o2, o3;
            asm volatile("global_load_dwordx4 %0, %4, %5 sc1\n\tglobal_load_dwordx4 %1, %4, %5 offset:1024 sc1\n\t"
                         "global_load_dwordx4 %2, %4, %5 offset:2048 sc1\n\tglobal_load_dwordx4 %3, %4, %5 offset:3072 sc1\n\ts_waitcnt vmcnt(0)"
                         : "=&v"(o0), "=&v"(o1), "=&v"(o2), "=&v"(o3) : "v"(lane * 16), "s"(og) : "memory");
            LDSP char* od = sm + L3_O + 4 * w * 1024 + lane * 16;
            *(LDSP u32x4*)(od) = o0; *(LDSP u32x4*)(od + 1024) = o1; *(LDSP u32x4*)(od + 2048) = o2; *(LDSP u32x4*)(od + 3072) = o3;
        }
        BAR_LGKM();
        if (b == 0 && w == 4 && lane == 0) __hip_atomic_store((gu64*)(ws + WS_EP), tag, RLX_AGENT);
    } else {
        const int hw = w - 8, c = hw >> 1, cp = 32 * (b & 7) + (b >> 3), ch = 2 * cp + c, n = (hw & 1) * 64 + lane, ht = threadIdx.x - 512;
        const int xg = b & 7, xj = b >> 3;
        LDSP unsigned* const cnt = (LDSP unsigned*)(sm + L_FLAG + 32);
        auto helper_sync = [&](int i) { WAIT_LGKM(); if (lane == 0) __hip_atomic_fetch_add(cnt + i, 1u, __ATOMIC_RELAXED, __HIP_MEMORY_SCOPE_WORKGROUP); wait_lds_word((const LDSP volatile unsigned*)(cnt + i), 4); };
        unsigned p_logA, p_aim, p_cre, p_cim, p_ldt;
        u32x4 ut[4] = {};
        {
            const unsigned po = (ch * 128 + n) * 2;
            asm volatile("global_load_ushort %0, %5, %7\n\tglobal_load_ushort %1, %5, %8\n\tglobal_load_ushort %2, %5, %9\n\tglobal_load_ushort %3, %5, %10\n\t"
                         "global_load_ushort %4, %6, %11"
                         : "=&v"(p_logA), "=&v"(p_aim), "=&v"(p_cre), "=&v"(p_cim), "=&v"(p_ldt)
                         : "v"(po), "v"(ch * 2), "s"(logA), "s"(aim), "s"(cre), "s"(cim), "s"(logdt) : "memory");
            if (hw >= 2) {
                const char* ug = (const char*)u + (size_t)(64 * xj + 32 * (hw - 2)) * 1024 + xg * 128;
                const unsigned uo = (lane >> 3) * 1024 + (lane & 7) * 16;
#pragma unroll
                for (int i = 0; i < 4; ++i) asm volatile("global_load_dwordx4 %0, %1, %2" : "+v"(ut[i]) : "v"(uo), "s"(ug + i * 8192) : "memory");
            }
        }
        const int rot = head & 7;
        float xl = 0.f, yr = 0.f; int ex = 0;
        cplx C = {0.f, 0.f}, z1 = C, z2 = C, z4 = C, z8 = C, z256 = C, vb = {1.f, 0.f};
        unsigned bpk[32], apk[21];
        {
            unsigned xoff[4];
#pragma unroll
            for (int i = 0; i < 4; ++i) { const int rr = 8 * i + (lane >> 3); xoff[i] = rr * 1024 + (((lane & 7) ^ ((rr >> 1) & 7)) << 4); }
            const char* xb = (const char*)x + (size_t)(rb * 256 + hw * 64) * 1024;
            const unsigned wqk_o = (lane >> 2) * 3072 + ((lane >> 1) & 1) * 1024 + (lane & 1) * 16, wv_o = (lane >> 1) * 3072 + 2048 + (lane & 1) * 16;
            const char* wq_b = (const char*)wqkv + head * 32 + (size_t)hw * 16 * 3072;
            const char* wv_b = (const char*)wqkv + head * 32 + (size_t)hw * 32 * 3072;
            auto issue = [&](int s) {
                const int sr = (s + rot) & 7;
                const unsigned slot = lds0 + (s & 3) * STAGE;
                glds16(wq_b + (size_t)sr * 64 * 3072, wqk_o, slot + ST_W + hw * 1024);
                if (hw < 2) glds16(wv_b + (size_t)sr * 64 * 3072, wv_o, slot + ST_WV + hw * 1024);
#pragma unroll
                for (int i = 0; i < 8; ++i) glds16(xb + (size_t)(i >> 2) * 32 * 1024 + sr * 128, xoff[i & 3], slot + hw * 8192 + i * 1024);
            };
            issue(0);
            issue(1);
#pragma unroll
            for (int s = 0; s < 6; ++s) {
                if (s == 0) WAIT_BAR(9); else WAIT_BAR(18);
                if (s == 0) issue(2);
                if (s + 3 < 8) issue(s + 3);
                if (s == 1) {
                    asm volatile("" : "+v"(p_logA), "+v"(p_aim), "+v"(p_cre), "+v"(p_cim), "+v"(p_ldt));
                    const float dt = bf16r(expf(bf2f(p_ldt)));
                    xl = -bf16r(expf(bf2f(p_logA))) * dt * 1.4426950408889634f;
                    yr = bf2f(p_aim) * dt * 0.15915494309189535f;
                    C.r = 2.0f * (bf2f(p_cre) * dt); C.i = 2.0f * (bf2f(p_cim) * dt);
                    ex = __builtin_amdgcn_frexp_expf(wave_max(fmaxf(fabsf(C.r), fabsf(C.i))));
                    C.r = ldexpf(C.r, -ex); C.i = ldexpf(C.i, -ex);
                    asm volatile("" : "+v"(xl), "+v"(yr), "+v"(C.r), "+v"(C.i));
                }
                if (s == 2) {
                    z1 = zpow(xl, yr, 1.f); z8 = zpow(xl, yr, 8.f); z256 = zpow(xl, yr, 256.f); z2 = cmul(z1, z1); z4 = cmul(z2, z2);
                    asm volatile("" : "+v"(z1.r), "+v"(z1.i), "+v"(z8.r), "+v"(z8.i), "+v"(z256.r), "+v"(z256.i), "+v"(z2.r), "+v"(z2.i), "+v"(z4.r), "+v"(z4.i));
                }
                if (s >= 3) {
#pragma unroll
                    for (int i = 0; i < 32; ++i)
                        if (i >= (s == 3 ? 0 : s == 4 ? 11 : 22) && i < (s == 3 ? 11 : s == 4 ? 22 : 32)) { bpk[i] = cvtpk_f16(vb.r, vb.i); vb = cmul(vb, z8); asm volatile("" : "+v"(bpk[i])); }
                    asm volatile("" : "+v"(vb.r), "+v"(vb.i));
                }
                if (s == 5) {
                    cplx a = C;
#pragma unroll
                    for (int i = 0; i < 8; ++i) { apk[i] = cvtpk_f16(a.r, -a.i); a = cmul(a, z1); asm volatile("" : "+v"(apk[i])); }
                }
            }
        }
        asm volatile("" : "+v"(ut[0]), "+v"(ut[1]), "+v"(ut[2]), "+v"(ut[3]));
        const unsigned long long tag = get_tag();
        WAIT_BAR(9);
        {
            cplx gk = cmul(C, z256), a = gk;
#pragma unroll
            for (int i = 0; i < 4; ++i) { apk[8 + i] = cvtpk_f16(a.r, -a.i); a = cmul(a, z2); }
            gk = cmul(gk, z256); apk[12] = cvtpk_f16(gk.r, -gk.i); a = cmul(gk, z4); apk[13] = cvtpk_f16(a.r, -a.i);
            gk = cmul(gk, z256); apk[14] = cvtpk_f16(gk.r, -gk.i); a = cmul(gk, z4); apk[15] = cvtpk_f16(a.r, -a.i);
#pragma unroll
            for (int i = 0; i < 5; ++i) { gk = cmul(gk, z256); apk[16 + i] = cvtpk_f16(gk.r, -gk.i); }
#pragma unroll
            for (int i = 8; i < 21; ++i) asm volatile("" : "+v"(apk[i]));
        }
        if (hw >= 2) {
            LDSP char* st = sm + L_UST + (hw - 2) * 4096;
#pragma unroll
            for (int i = 0; i < 4; ++i) *(LDSP u32x4*)(st + i * 1024 + lane * 16) = ut[i];
            const int q = (lane & 15) >> 2;
            const LDSP char* tp = st + (lane >> 4) * 32 + (lane & 3) * 8;
#pragma unroll
            for (int k = 0; k < 4; ++k) {
                const s16x4 hi = __builtin_amdgcn_ds_read_tr16_b64_v4i16((LDSP s16x4*)(tp + (8 * k + 7 - q) * 128));
                const s16x4 lo = __builtin_amdgcn_ds_read_tr16_b64_v4i16((LDSP s16x4*)(tp + (8 * k + 3 - q) * 128));
                const u32x2 a = __builtin_bit_cast(u32x2, hi), bb = __builtin_bit_cast(u32x2, lo);
                ut[k] = (u32x4){a[0], a[1], bb[0], bb[1]};
            }
#pragma unroll
            for (int k = 0; k < 4; ++k) *(LDSP u32x4*)(st + lane * 64 + k * 16) = ut[k];
#pragma unroll
            for (int k = 0; k < 4; ++k) ut[k] = *(const LDSP u32x4*)(st + k * 1024 + lane * 16);
        }
        WAIT_BAR(0);
        if (hw >= 2) {
#pragma unroll
            for (int k = 0; k < 4; ++k) {
                const int chn = 16 * k + (lane >> 2);
                const int blk = 2 * xj + hw - 2;
                store16_dev_v(ws + WS_UT + (size_t)(64 * xg + chn) * 5120 + ((chn & 1) ? 63 - blk : blk) * UBLK + 16 * (lane & 3), ut[k]);
            }
        }
        int ci[9];
#pragma unroll
        for (int e = 0; e < 9; ++e) { const int j = 8 * ht - 1 + e; const int bits = (int)(cvtpk_bf16((float)j, 0.f) & 0xffffu); ci[e] = 4 * tab_idx(j < 128 ? (j < 0 ? 0 : j) : bits - 17024); asm volatile("" : "+v"(ci[e])); }
        BAR_LGKM();
        __builtin_amdgcn_s_setprio(3);
        if (ht < 16) *(LDSP unsigned*)(sm + L_FLAG + ht * 4) = 0;
        BAR_LGKM();
        if (hw < 2) {
            const int T0 = rb * 8 + 4 * hw;
            u32x4 kk[4], vv[4];
#pragma unroll
            for (int i = 0; i < 4; ++i) { kk[i] = *(const LDSP u32x4*)(sm + L_K + (T0 + i) * 1024 + lane * 16); vv[i] = *(const LDSP u32x4*)(sm + L_V + (T0 + i) * VT + lane * 16); }
            char* kg = ws + WS_K + ((size_t)head * 64 + T0) * 1024;
            char* vg = ws + WS_V + ((size_t)head * 64 + T0) * 1024;
#pragma unroll
            for (int i = 0; i < 4; ++i) { store16_dev(kg + i * 1024, lane * 16, kk[i]); store16_dev(vg + i * 1024, lane * 16, vv[i]); }
        }
        BAR_LGKM();
        if (hw == 0) { const float kmp = *(const LDSP float*)(sm + L_QX + 8192 + (lane & 7) * 4); if (lane < 8) store4_dev(ws + WS_KM, (head * 64 + rb * 8 + lane) * 4, kmp); }
        BAR_LGKM();
        auto signal = [&]() {
            WAIT_VM(0);
            if (lane == 0 && __hip_atomic_fetch_add(cnt + (hw < 2 ? 1 : 4), 1u, __ATOMIC_RELAXED, __HIP_MEMORY_SCOPE_WORKGROUP) == 1)
                __hip_atomic_store((gu64*)(ws + (hw < 2 ? WS_F1 + wg * 128 : WS_F0 + b * 128)), tag, RLX_AGENT);
        };
        signal();
        unsigned wpf = 0;
        if (hw == 3) {
            asm volatile("global_load_dword %0, %1, %2\n\tglobal_load_dword %0, %1, %2 offset:1024" : "+v"(wpf) : "v"((unsigned)(16 * xj + 2 * (lane >> 3)) * 1024 + (lane & 7) * 128), "s"(wo) : "memory");
        }
        auto treg = [&](int i) { return L_K + 8192 * ((rb + 4 + i) & 7); };
        LDSP char* buf = sm + treg(hw);
        LDSP unsigned* row = (LDSP unsigned*)(buf + lane * 4);
        const LDSP char* frag = buf + (r & 15) * P_ROW + h * 16;
        f16x8 bfr[8], afr[8];
#pragma unroll
        for (int i = 0; i < 16; ++i) row[i * (P_ROW / 4)] = bpk[i];
#pragma unroll
        for (int t = 0; t < 8; ++t) bfr[t] = *(const LDSP f16x8*)(frag + t * 32);
#pragma unroll
        for (int i = 0; i < 16; ++i) row[i * (P_ROW / 4)] = bpk[16 + i];
        if (r >= 16) {
#pragma unroll
            for (int t = 0; t < 8; ++t) bfr[t] = *(const LDSP f16x8*)(frag + t * 32);
        }
#pragma unroll
        for (int i = 0; i < 16; ++i) row[i * (P_ROW / 4)] = apk[i];
#pragma unroll
        for (int t = 0; t < 8; ++t) afr[t] = *(const LDSP f16x8*)(frag + t * 32);
#pragma unroll
        for (int i = 0; i < 5; ++i) row[i * (P_ROW / 4)] = apk[16 + i];
        if (r >= 16) {
#pragma unroll
            for (int t = 0; t < 8; ++t) afr[t] = *(const LDSP f16x8*)(frag + t * 32);
        }
        f32x16 d = {};
#pragma unroll
        for (int t = 0; t < 8; ++t) d = mfma_f16(afr[t], bfr[t], d);
        {
            LDSP float* tc = (LDSP float*)(buf + P_BUF);
#pragma unroll
            for (int e = 0; e < 4; ++e) {
                tc[tab_idx(8 * r + 4 * h + e)] = ldexpf(d[e], ex);
                tc[tab_idx(h ? 384 + 64 * (e >> 1) + 2 * r + (e & 1) : 256 + 4 * r + e)] = ldexpf(d[4 + e], ex);
                if (h == 0) tc[tab_idx(512 + 32 * e + r)] = ldexpf(d[8 + e], ex);
            }
            if (lane == 32) tc[tab_idx(640)] = ldexpf(d[8], ex);
        }
        if (hw < 2) {
            const u32x4 z = {0, 0, 0, 0};
            for (int i = ht; i < 31 * 5; i += 128) *(LDSP u32x4*)(sm + L_Z + i * 16) = z;
        } else {
            wait_tags((const gu64*)(ws + WS_F0 + (xg + 8 * (lane & 31)) * 128), tag);
            const char* ug = ws + WS_UT + (size_t)(2 * cp + (hw - 2)) * 5120;
            const unsigned dst = lds0 + (hw == 2 ? L_U0 : L_U1);
#pragma unroll
            for (int i = 0; i < 5; ++i) glds16_dev(ug + i * 1024, lane * 16, dst + i * 1024);
        }
        helper_sync(7);
        {
#pragma unroll
            for (int cc = 0; cc < 2; ++cc) {
                float k[9];
#pragma unroll
                for (int e = 0; e < 9; ++e)
                    k[e] = *(const LDSP float*)(sm + treg(2 * cc) + P_BUF + ci[e]) + *(const LDSP float*)(sm + treg(2 * cc + 1) + P_BUF + ci[e]);
                if (ht == 0) k[0] = 0.f;
                const u32x4 c0 = {cvtpk_bf16(k[1], k[2]), cvtpk_bf16(k[3], k[4]), cvtpk_bf16(k[5], k[6]), cvtpk_bf16(k[7], k[8])};
                const u32x4 c1 = {cvtpk_bf16(k[0], k[1]), cvtpk_bf16(k[2], k[3]), cvtpk_bf16(k[4], k[5]), cvtpk_bf16(k[6], k[7])};
                *(LDSP u32x4*)(sm + L_TAP + cc * TAP_CH + (ht + 8) * 16) = c0;
                *(LDSP u32x4*)(sm + L_TAP + cc * TAP_CH + TAP_C1 + (ht + 8) * 16) = c1;
            }
            if (ht < 32) { const u32x4 z = {0, 0, 0, 0}; *(LDSP u32x4*)(sm + L_TAP + (ht >> 4) * TAP_CH + ((ht >> 3) & 1) * TAP_C1 + (ht & 7) * 16) = z; }
        }
        WAIT_VM(0);
        asm volatile("" : "+v"(wpf));
        helper_sync(3);
        {
            const int tile = hw & 1;
            const int e0 = r - 8 * h - 7 + 64;
            const unsigned ap = lds0 + L_TAP + c * TAP_CH + ((r & 1) ? 2 * e0 : TAP_C1 + 2 * (e0 + 1)) - 32;
            const int ustep = c ? (int)UBLK : -(int)UBLK;
            const int ib = 32 * tile + r;
            const unsigned bp = lds0 + (c ? L_Z - (ib + 1) * UBLK : L_U0 + ib * UBLK) + 16 * h;
            f32x16 y;
            {
                unsigned va = ap, vb0 = bp, vb1 = bp + ustep, trips = 16 << tile;
                asm volatile(CONV_ASM : "={v[40:55]}"(y), "+{v88}"(va), "+{v89}"(vb0), "+{v90}"(vb1), [m] "+s"(trips) : "{v91}"(2 * ustep)
                             : "v56", "v57", "v58", "v59", "v60", "v61", "v62", "v63", "v64", "v65", "v66", "v67", "v68", "v69", "v70", "v71", "v72", "v73",
                               "v74", "v75", "v76", "v77", "v78", "v79", "v80", "v81", "v82", "v83", "v84", "v85", "v86", "v87", "scc", "memory");
            }
            char* yg = ws + WS_YT + (size_t)(2 * cp + c) * 4096;
#pragma unroll
            for (int gp = 0; gp < 2; ++gp) {
                const unsigned a0 = cvtpk_bf16(y[8 * gp + 0], y[8 * gp + 1]), a1 = cvtpk_bf16(y[8 * gp + 2], y[8 * gp + 3]);
                const unsigned b0 = cvtpk_bf16(y[8 * gp + 4], y[8 * gp + 5]), b1 = cvtpk_bf16(y[8 * gp + 6], y[8 * gp + 7]);
                const auto s0 = __builtin_amdgcn_permlane32_swap(a0, b0, false, false);
                const auto s1 = __builtin_amdgcn_permlane32_swap(a1, b1, false, false);
                const u32x4 v = {s0[0], s1[0], s0[1], s1[1]};
                store16_dev(yg, tile * 2048 + r * 64 + gp * 32 + h * 16, v);
            }
        }
        __builtin_amdgcn_s_setprio(2);
        WAIT_VM(0);
        if (lane == 0 && __hip_atomic_fetch_add(cnt + 6, 1u, __ATOMIC_RELAXED, __HIP_MEMORY_SCOPE_WORKGROUP) == 3)
            __hip_atomic_store((gu64*)(ws + WS_F3 + b * 128), tag, RLX_AGENT);
        helper_sync(5);
        {
            const unsigned yb = hw < 3 ? L_TAP + hw * 4096 : L_U1, ub = yb + 2048;
            const char* ut = (const char*)u + (size_t)(64 * xj + 16 * hw) * 1024 + xg * 128;
            const unsigned uo = (lane >> 3) * 1024 + (lane & 7) * 16;
            glds16(ut, uo, lds0 + ub); glds16(ut + 8 * 1024, uo, lds0 + ub + 1024);
            const int i = lane & 15, grp = lane >> 4;
            u32x4 dk[2];
#pragma unroll
            for (int it = 0; it < 2; ++it) dk[it] = *(const u32x4*)((const char*)dskip + (64 * xg + 8 * (4 * it + grp)) * 2);
            wait_tags((const gu64*)(ws + WS_F3 + (xg + 8 * (lane & 31)) * 128), tag);
            const char* yg = ws + WS_YT + (size_t)(64 * xg) * 4096 + (64 * xj + 16 * hw) * 2;
            const unsigned yo = (lane >> 1) * 4096 + (lane & 1) * 16;
            glds16_dev(yg, yo, lds0 + yb); glds16_dev(yg + 32 * 4096, yo, lds0 + yb + 1024);
            WAIT_VM(0);
            const int q = i >> 2, p = lane & 3;
#pragma unroll
            for (int it = 0; it < 2; ++it) {
                const int kap = 4 * it + grp;
                const s16x4 y0 = __builtin_amdgcn_ds_read_tr16_b64_v4i16((LDSP s16x4*)(sm + yb + (8 * kap + q) * 32 + 8 * p));
                const s16x4 y1 = __builtin_amdgcn_ds_read_tr16_b64_v4i16((LDSP s16x4*)(sm + yb + (8 * kap + 4 + q) * 32 + 8 * p));
                const u32x4 uu = *(const LDSP u32x4*)(sm + ub + i * 128 + 16 * kap);
                const u32x2 ya = __builtin_bit_cast(u32x2, y0), yc = __builtin_bit_cast(u32x2, y1);
                const u32x4 yy = {ya[0], ya[1], yc[0], yc[1]};
                u32x4 o;
#pragma unroll
                for (int e = 0; e < 4; ++e) {
                    const float ulo = __uint_as_float(uu[e] << 16), uhi = __uint_as_float(uu[e] & 0xffff0000u);
                    const float dlo = __uint_as_float(dk[it][e] << 16), dhi = __uint_as_float(dk[it][e] & 0xffff0000u);
                    const unsigned sk = cvtpk_bf16(ulo * dlo, uhi * dhi);
                    o[e] = cvtpk_bf16(__uint_as_float(yy[e] << 16) + __uint_as_float(sk << 16),
                                      __uint_as_float(yy[e] & 0xffff0000u) + __uint_as_float(sk & 0xffff0000u));
                }
                *(u32x4*)((char*)out + (size_t)(64 * xj + 16 * hw + i) * 2048 + (64 * xg + 8 * kap) * 2) = o;
            }
        }
        const int fg = head & 3, qt3 = rb * 8 + 2 * (head >> 3) + ((head >> 2) & 1);
        u32x16 wt[8];
        {
            const unsigned wb_ = hw < 3 ? L_TAP + hw * 4096 : L_U1;
            const char* wg_ = (const char*)wo + fg * 256 + hw * 64;
            const unsigned woff = (lane >> 2) * 1024 + (lane & 3) * 16;
            const LDSP char* ap = sm + wb_ + (4 * h + ((lane & 15) >> 2)) * 64 + (((lane >> 4) & 1) * 16 + (lane & 3) * 4) * 2;
            WAIT_LGKM();
#pragma unroll
            for (int g = 0; g < 8; ++g) {
#pragma unroll
                for (int i = 0; i < 4; ++i) glds16(wg_ + (size_t)(4 * g + i) * 16384, woff, lds0 + wb_ + i * 1024);
                WAIT_VM(0);
#pragma unroll
                for (int i = 0; i < 4; ++i) {
                    const u32x4 f = __builtin_bit_cast(u32x4, tr_frag(ap, i * 1024, i * 1024 + 512));
                    wt[g][4 * i] = f[0]; wt[g][4 * i + 1] = f[1]; wt[g][4 * i + 2] = f[2]; wt[g][4 * i + 3] = f[3];
                }
                if (g == 0) asm volatile("s_waitcnt lgkmcnt(0)" : "+{v[32:47]}"(wt[0]) :: "memory");
                if (g == 1) asm volatile("s_waitcnt lgkmcnt(0)" : "+{v[48:63]}"(wt[1]) :: "memory");
                if (g == 2) asm volatile("s_waitcnt lgkmcnt(0)" : "+{v[64:79]}"(wt[2]) :: "memory");
                if (g == 3) asm volatile("s_waitcnt lgkmcnt(0)" : "+{v[80:95]}"(wt[3]) :: "memory");
                if (g == 4) asm volatile("s_waitcnt lgkmcnt(0)" : "+{v[96:111]}"(wt[4]) :: "memory");
                if (g == 5) asm volatile("s_waitcnt lgkmcnt(0)" : "+{v[112:127]}"(wt[5]) :: "memory");
                if (g == 6) asm volatile("s_waitcnt lgkmcnt(0)" : "+{v[128:143]}"(wt[6]) :: "memory");
                if (g == 7) asm volatile("s_waitcnt lgkmcnt(0)" : "+{v[144:159]}"(wt[7]) :: "memory");
            }
        }
        BAR_LGKM();
        asm volatile("s_barrier" ::: "memory");
        asm volatile("s_barrier" ::: "memory");
        {
            f32x16 acc;
            asm volatile(PROJ_ASM : "={v[0:15]}"(acc), "+{v[32:47]}"(wt[0]), "+{v[48:63]}"(wt[1]), "+{v[64:79]}"(wt[2]), "+{v[80:95]}"(wt[3]),
                                    "+{v[96:111]}"(wt[4]), "+{v[112:127]}"(wt[5]), "+{v[128:143]}"(wt[6]), "+{v[144:159]}"(wt[7])
                         : "{v16}"(lds0 + L3_O + lane * 16)
                         : "v20", "v21", "v22", "v23", "v24", "v25", "v26", "v27", "v28", "v29", "v30", "v31", "memory");
            char* op = (char*)out + (size_t)(qt3 * 32 + r) * 2048 + 1024 + (fg * 128 + hw * 32) * 2 + h * 16;
#pragma unroll
            for (int gp = 0; gp < 2; ++gp) {
                const unsigned a0 = cvtpk_bf16(acc[8 * gp + 0], acc[8 * gp + 1]), a1 = cvtpk_bf16(acc[8 * gp + 2], acc[8 * gp + 3]);
                const unsigned b0 = cvtpk_bf16(acc[8 * gp + 4], acc[8 * gp + 5]), b1 = cvtpk_bf16(acc[8 * gp + 6], acc[8 * gp + 7]);
                const auto s0 = __builtin_amdgcn_permlane32_swap(a0, b0, false, false);
                const auto s1 = __builtin_amdgcn_permlane32_swap(a1, b1, false, false);
                const u32x4 v = {s0[0], s1[0], s0[1], s1[1]};
                *(u32x4*)(op + gp * 32) = v;
            }
        }
    }
}

extern "C" void kernel_launch(void* const* d_in, const int* in_sizes, int n_in, void* d_out, int out_size, void* d_ws, size_t ws_size,
                              hipStream_t stream) {
    kf_fused<<<256, 768, 0, stream>>>((const unsigned short*)d_in[0], (const unsigned short*)d_in[1], (const unsigned short*)d_in[2],
                                      (const unsigned short*)d_in[3], (const unsigned short*)d_in[4], (const unsigned short*)d_in[5],
                                      (const unsigned short*)d_in[6], (const unsigned short*)d_in[7], (const unsigned short*)d_in[8],
                                      (const unsigned short*)d_in[9], (char*)d_ws, (unsigned short*)d_out);
}
```
